```python
import torch

def _softmax(x, axis=-1):
    return torch.softmax(x, dim=axis)
def _gelu(x):
    return torch.nn.functional.gelu(x, approximate="tanh")
def _silu(x):
    return torch.nn.functional.silu(x)
def _sigmoid(x):
    return torch.sigmoid(x)
def _layernorm(x, axis=-1, eps=1e-5):
    m = x.mean(dim=axis, keepdim=True)
    v = x.var(dim=axis, keepdim=True, unbiased=False)
    return (x - m) / torch.sqrt(v + eps)
def _scatter_add(tgt, idx, src, axis=0):
    return tgt.clone().index_add_(axis, idx, src)
def _segment_sum(src, seg, n):
    out = torch.zeros((n,) + src.shape[1:], dtype=src.dtype)
    return out.index_add_(0, seg, src)
def _pad(x, pw):
    flat = tuple(v for pair in reversed(pw) for v in pair)
    return torch.nn.functional.pad(x, flat)

kernel_name = 'sequential_preLN__sliding_block__glu_gated_reglu__prompt'

def setup_inputs():

    _w = (lambda a: (lambda t: t.to(torch.bfloat16) if t.is_floating_point() else t)(torch.as_tensor(a)))
    def _A_setup():
        B, S, D = 1, 2048, 640
        return {
            "x": _w((lambda *s: torch.randn(*s, dtype=torch.bfloat16) * 0.05)(B, S, D)),
            "wqkv": _w((lambda *s: torch.randn(*s, dtype=torch.bfloat16) * 0.05)(D, 3 * D)),
            "wo": _w((lambda *s: torch.randn(*s, dtype=torch.bfloat16) * 0.05)(D, D)),
        }
    _A_in = _A_setup()

    def _B_setup():
        B, S, D, F = 1, 2048, 640, 1728
        return {
            "x": _w((lambda *s: torch.randn(*s, dtype=torch.bfloat16) * 0.05)(B, S, D)),
            "W_gate": _w((lambda *s: torch.randn(*s, dtype=torch.bfloat16) * 0.8)(D, F) * (2.0 / D) ** 0.5),
            "W_up": _w((lambda *s: torch.randn(*s, dtype=torch.bfloat16) * 0.8)(D, F) * (2.0 / D) ** 0.5),
            "W_down": _w((lambda *s: torch.randn(*s, dtype=torch.bfloat16) * 0.8)(F, D) * (1.0 / F) ** 0.5),
        }
    _B_in = _B_setup()

    return {
        "A_x": _A_in["x"],
        "A_wqkv": _A_in["wqkv"],
        "A_wo": _A_in["wo"],
        "B_x": _B_in["x"],
        "B_W_gate": _B_in["W_gate"],
        "B_W_up": _B_in["W_up"],
        "B_W_down": _B_in["W_down"],
    }

def reference(A_x, A_wqkv, A_wo, B_x, B_W_gate, B_W_up, B_W_down):
    def _A_ref(x, wqkv, wo):
        B, S, D = x.shape
        H = 32; Dh = D // H; W = 256
        qkv = (lambda x, *d: x.permute(*d))((x @ wqkv).reshape(B, S, 3, H, Dh), 2, 0, 3, 1, 4)
        q, k, v = qkv[0], qkv[1], qkv[2]
        scores = torch.einsum("bhqd,bhkd->bhqk", q, k) * (Dh ** -0.5)
        idx = torch.arange(S)
        mask = (idx[:, None] - idx[None, :] >= 0) & (idx[:, None] - idx[None, :] < W)
        scores = torch.where(mask[None, None], scores, -1e9)
        p = _softmax(scores, axis=-1)
        o = torch.einsum("bhqk,bhkd->bhqd", p, v)
        return (lambda x, *d: x.permute(*d))(o, 0, 2, 1, 3).reshape(B, S, D) @ wo

    def _B_ref(x, W_gate, W_up, W_down):
        gate = torch.einsum("bsd,df->bsf", x, W_gate)
        up = torch.einsum("bsd,df->bsf", x, W_up)
        gate = gate * (lambda x, dt: x.to(dt))(gate > 0, gate.dtype)
        h = gate * up
        return torch.einsum("bsf,fd->bsd", h, W_down)

    x = A_x
    h = x + _A_ref(_layernorm(x, axis=-1), A_wqkv, A_wo)
    h2 = h + _B_ref(_layernorm(h, axis=-1), B_W_gate, B_W_up, B_W_down)
    return h2
```

```cpp
#include <hip/hip_runtime.h>
#include <hip/hip_bf16.h>
typedef unsigned short u16;
__device__ __forceinline__ float bf2f(u16 v){ return __uint_as_float(((unsigned)v)<<16); }
__device__ __forceinline__ u16 f2bf(float f){ unsigned u=__float_as_uint(f); return (u16)((u + 0x7FFF + ((u>>16)&1))>>16); }

constexpr int S=2048, D=640, H=32, DH=20, W=256, F=1728;

__global__ void ln_kernel(const u16* x, u16* y){
  int row=blockIdx.x; int t=threadIdx.x; __shared__ float red[256];
  float s=0; for(int i=t;i<D;i+=256) s+=bf2f(x[row*D+i]);
  red[t]=s; __syncthreads();
  for(int o=128;o>0;o>>=1){ if(t<o) red[t]+=red[t+o]; __syncthreads(); }
  float m=red[0]/D; __syncthreads();
  float q=0; for(int i=t;i<D;i+=256){ float d=bf2f(x[row*D+i])-m; q+=d*d; }
  red[t]=q; __syncthreads();
  for(int o=128;o>0;o>>=1){ if(t<o) red[t]+=red[t+o]; __syncthreads(); }
  float r=rsqrtf(red[0]/D+1e-5f);
  for(int i=t;i<D;i+=256) y[row*D+i]=f2bf((bf2f(x[row*D+i])-m)*r);
}
__global__ void gemm_kernel(const u16* A, const u16* B, u16* C, const u16* res, int M, int N, int K){
  int n=blockIdx.x*64+threadIdx.x; int m=blockIdx.y*4+threadIdx.y;
  if(n>=N) return;
  float acc=0; for(int k=0;k<K;k++) acc+=bf2f(A[m*K+k])*bf2f(B[k*N+n]);
  u16 r=f2bf(acc);
  if(res) r=f2bf(bf2f(r)+bf2f(res[m*N+n]));
  C[m*N+n]=r;
}
__global__ void attn_kernel(const u16* qkv, u16* o){
  int i=blockIdx.x; int h=blockIdx.y; int t=threadIdx.x;
  __shared__ float p[256]; __shared__ float red[256];
  int j=i-t; float s=-1e30f;
  if(j>=0){ float a=0; for(int d=0;d<DH;d++) a+=bf2f(qkv[i*3*D+h*DH+d])*bf2f(qkv[j*3*D+D+h*DH+d]); s=a*0.22360679775f; }
  red[t]=s; __syncthreads();
  for(int o2=128;o2>0;o2>>=1){ if(t<o2) red[t]=fmaxf(red[t],red[t+o2]); __syncthreads(); }
  float mx=red[0]; __syncthreads();
  float e=(j>=0)?__expf(s-mx):0.f; p[t]=e; red[t]=e; __syncthreads();
  for(int o2=128;o2>0;o2>>=1){ if(t<o2) red[t]+=red[t+o2]; __syncthreads(); }
  float sum=red[0];
  if(t<DH){ float a=0; for(int u=0;u<256;u++){ int jj=i-u; if(jj>=0) a+=bf2f(f2bf(p[u]/sum))*bf2f(qkv[jj*3*D+2*D+h*DH+t]); } o[i*D+h*DH+t]=f2bf(a); }
}
__global__ void glu_kernel(const u16* g, const u16* u, u16* h, int n){
  int i=blockIdx.x*256+threadIdx.x; if(i<n){ float a=bf2f(g[i]); a=a>0?a:0; h[i]=f2bf(a*bf2f(u[i])); }
}
extern "C" void kernel_launch(void* const* d_in, const int* in_sizes, int n_in, void* d_out, int out_size, void* d_ws, size_t ws_size, hipStream_t stream){
  const u16* x=(const u16*)d_in[0]; const u16* wqkv=(const u16*)d_in[1]; const u16* wo=(const u16*)d_in[2];
  const u16* wg=(const u16*)d_in[4]; const u16* wu=(const u16*)d_in[5]; const u16* wd=(const u16*)d_in[6];
  u16* ws=(u16*)d_ws; u16* ln=ws; u16* qkv=ln+S*D; u16* o=qkv+S*3*D; u16* h=o+S*D; u16* g=h+S*D; u16* u=g+S*F; u16* hh=u+S*F;
  dim3 b(64,4);
  ln_kernel<<<S,256,0,stream>>>(x,ln);
  gemm_kernel<<<dim3(3*D/64,S/4),b,0,stream>>>(ln,wqkv,qkv,nullptr,S,3*D,D);
  attn_kernel<<<dim3(S,H),256,0,stream>>>(qkv,o);
  gemm_kernel<<<dim3(D/64,S/4),b,0,stream>>>(o,wo,h,x,S,D,D);
  ln_kernel<<<S,256,0,stream>>>(h,ln);
  gemm_kernel<<<dim3(F/64,S/4),b,0,stream>>>(ln,wg,g,nullptr,S,F,D);
  gemm_kernel<<<dim3(F/64,S/4),b,0,stream>>>(ln,wu,u,nullptr,S,F,D);
  glu_kernel<<<(S*F+255)/256,256,0,stream>>>(g,u,hh,S*F);
  gemm_kernel<<<dim3(D/64,S/4),b,0,stream>>>(hh,wd,(u16*)d_out,h,S,D,F);
}
```

```cpp
#include <hip/hip_runtime.h>
#include <stdint.h>

typedef unsigned short u16;
typedef unsigned int u32;
typedef unsigned long long u64;
typedef short s16x4 __attribute__((ext_vector_type(4)));
typedef short s16x8 __attribute__((ext_vector_type(8)));
typedef __bf16 bf16x8 __attribute__((ext_vector_type(8)));
typedef __bf16 bf16x2 __attribute__((ext_vector_type(2)));
typedef float f32x2 __attribute__((ext_vector_type(2)));
typedef float f32x4 __attribute__((ext_vector_type(4)));
typedef float f32x16 __attribute__((ext_vector_type(16)));
typedef u32 u32x2 __attribute__((ext_vector_type(2)));
typedef u32 u32x4 __attribute__((ext_vector_type(4)));
#define LAS __attribute__((address_space(3)))
#define GAS __attribute__((address_space(1)))
#define DEV __device__ __forceinline__

#define NLAUNCH 1
#define STAMPS 0

constexpr int SEQ = 2048, DM = 640, FF = 1728;
constexpr u32 WS_EPOCH = 0;
constexpr u32 WS_FLAGS = 4096;
constexpr u32 FL_QKV = 0, FL_ATT = 256, FL_H = 512, FL_FF = 768, FL_CS = 1000;
constexpr u32 WS_QKV = 256 * 1024;
constexpr u32 SZ_QKV = 480 * 16384;
constexpr u32 WS_O = WS_QKV + SZ_QKV;
constexpr u32 SZ_O = 160 * 16384;
constexpr u32 WS_H = WS_O + SZ_O;
constexpr u32 SZ_H = 80 * 32768;
constexpr u32 WS_HST = WS_H + SZ_H;
constexpr u32 SZ_HST = 8 * 10 * 256 * 8;
constexpr u32 WS_G = WS_HST + SZ_HST;
constexpr u32 SZ_G = 216 * 32768;
constexpr u32 WS_CS = WS_G + SZ_G;
constexpr u32 SZ_CS = 54 * 64 * 4;
constexpr u32 WS_STAMP = WS_CS + 16384;
constexpr u32 LDS_BYTES = 160 * 1024;
constexpr u32 L_TAB = 156 * 1024;
constexpr u32 OOB = 0x80000000u;

DEV u32x4 mk_rsrc(const void* p, u32 bytes) {
  const u64 a = (u64)p;
  u32x4 r; r[0] = (u32)a; r[1] = (u32)(a >> 32) & 0xffffu; r[2] = bytes; r[3] = 0x00020000u; return r;
}
DEV __amdgpu_buffer_rsrc_t mk_rs(const void* p, u32 bytes) { return __builtin_amdgcn_make_buffer_rsrc((void*)p, 0, bytes, 0x00020000); }
DEV void dma(u32x4 rs, u32 voff, u32 soff, u32 lds) {
  asm volatile("s_mov_b32 m0, %0\n\ts_nop 0\n\tbuffer_load_dwordx4 %1, %2, %3 offen lds" ::"s"(lds), "v"(voff), "s"(rs), "s"(soff));
}
DEV void dma_c(u32x4 rs, u32 voff, u32 soff, u32 lds) {
  asm volatile("s_mov_b32 m0, %0\n\ts_nop 0\n\tbuffer_load_dwordx4 %1, %2, %3 offen sc1 lds" ::"s"(lds), "v"(voff), "s"(rs), "s"(soff));
}
#define WAITV(n) asm volatile("s_waitcnt vmcnt(" #n ")" ::: "memory")
#define BAR() asm volatile("s_waitcnt lgkmcnt(0)\n\ts_barrier" ::: "memory")
DEV u32 pk_bf16(float lo, float hi) { f32x2 v = {lo, hi}; return __builtin_bit_cast(u32, __builtin_convertvector(v, bf16x2)); }
DEV float bf_lo(u32 d) { return __uint_as_float(d << 16); }
DEV float bf_hi(u32 d) { return __uint_as_float(d & 0xffff0000u); }
DEV float rbf(float x) { return bf_lo(pk_bf16(x, x)); }
DEV s16x8 lds_b128(u32 a) { return *(LAS s16x8*)(size_t)a; }
DEV s16x4 lds_b64(u32 a) { return *(LAS s16x4*)(size_t)a; }
DEV s16x4 lds_tr(u32 a) { return __builtin_amdgcn_ds_read_tr16_b64_v4i16((LAS s16x4*)(size_t)a); }
DEV s16x8 cat(s16x4 a, s16x4 b) { return __builtin_shufflevector(a, b, 0, 1, 2, 3, 4, 5, 6, 7); }
DEV f32x4 mfma16(s16x8 a, s16x8 b, f32x4 c) { return __builtin_amdgcn_mfma_f32_16x16x32_bf16(__builtin_bit_cast(bf16x8, a), __builtin_bit_cast(bf16x8, b), c, 0, 0, 0); }
DEV f32x16 mfma32(s16x8 a, s16x8 b, f32x16 c) { return __builtin_amdgcn_mfma_f32_32x32x16_bf16(__builtin_bit_cast(bf16x8, a), __builtin_bit_cast(bf16x8, b), c, 0, 0, 0); }
DEV f32x16 mfma32k8(s16x4 a, s16x4 b, f32x16 c) { return __builtin_amdgcn_mfma_f32_32x32x8bf16_1k(a, b, c, 0, 0, 0); }
DEV float dot2(u32 a, u32 b, float c) { return __builtin_amdgcn_fdot2_f32_bf16(__builtin_bit_cast(bf16x2, a), __builtin_bit_cast(bf16x2, b), c, false); }
DEV float xhalf_max(float m) { auto r = __builtin_amdgcn_permlane32_swap(__float_as_uint(m), __float_as_uint(m), false, false); return fmaxf(__uint_as_float(r[0]), __uint_as_float(r[1])); }
DEV float sum_x16_x32(float s) { s += __shfl_xor(s, 16); s += __shfl_xor(s, 32); return s; }
DEV void ln_stats(float mean, float var, float& m_out, float& r_out) {
  m_out = rbf(mean);
  const float sd = rbf(__builtin_sqrtf(rbf(rbf(var) + 1e-5f)));
  r_out = 1.0f / sd;
}

struct Sync {
  GAS u64* flags; u64 tag;
  DEV void wait(int idx) const {
    GAS u64* p = flags + (idx < 0 ? 0 : idx) * 16;
    for (u32 spins = 0; spins < (1u << 16); ++spins) {
      const u64 v = __hip_atomic_load(p, __ATOMIC_RELAXED, __HIP_MEMORY_SCOPE_AGENT);
      if (__all(idx < 0 || v == tag)) break;
      __builtin_amdgcn_s_sleep(1);
    }
  }
  DEV void post(int idx) const { __hip_atomic_store(flags + idx * 16, tag, __ATOMIC_RELAXED, __HIP_MEMORY_SCOPE_AGENT); }
};

#define STAMP(i) do {} while (0)

DEV void colsum_job(const u16* wg, const u16* wu, char* ws, int G0, u32 lds0, int tid, int lane, int w) {
  u32x4 v[2][10];
#pragma unroll
  for (int sg = 0; sg < 2; ++sg) {
    int G = G0 + sg; if (G > 53) G = 53;
    const u16* W = (G < 27 ? wg + G * 64 : wu + (G - 27) * 64) + (lane & 7) * 8 + (size_t)(8 * w + (lane >> 3)) * FF;
#pragma unroll
    for (int i = 0; i < 10; ++i) v[sg][i] = *(const u32x4*)(W + (size_t)i * 64 * FF);
  }
#pragma unroll
  for (int sg = 0; sg < 2; ++sg) {
    float a[8];
#pragma unroll
    for (int e = 0; e < 8; ++e) a[e] = 0.f;
#pragma unroll
    for (int i = 0; i < 10; ++i)
#pragma unroll
      for (int e = 0; e < 4; ++e) { a[2 * e] += bf_lo(v[sg][i][e]); a[2 * e + 1] += bf_hi(v[sg][i][e]); }
#pragma unroll
    for (int e = 0; e < 8; ++e) { float s = a[e]; s += __shfl_xor(s, 8); s = sum_x16_x32(s); a[e] = s; }
    if (lane < 8) {
      LAS f32x4* p = (LAS f32x4*)(size_t)(lds0 + ((sg * 8 + w) * 64 + lane * 8) * 4);
      p[0] = (f32x4){a[0], a[1], a[2], a[3]}; p[1] = (f32x4){a[4], a[5], a[6], a[7]};
    }
  }
  BAR();
  if (tid < 128) {
    const int sg = tid >> 6, c = tid & 63; float s = 0.f;
#pragma unroll
    for (int ww = 0; ww < 8; ++ww) s += *(LAS float*)(size_t)(lds0 + ((sg * 8 + ww) * 64 + c) * 4);
    if (G0 + sg < 54) __hip_atomic_store((GAS float*)(ws + WS_CS) + (G0 + sg) * 64 + c, s, __ATOMIC_RELAXED, __HIP_MEMORY_SCOPE_AGENT);
  }
  WAITV(0);
  BAR();
}

__global__ __launch_bounds__(512, 2) void fused_block(const u16* __restrict__ x, const u16* __restrict__ wqkv, const u16* __restrict__ wo,
                                                      const u16* __restrict__ wg, const u16* __restrict__ wu, const u16* __restrict__ wd,
                                                      u16* __restrict__ out, char* __restrict__ ws, int ph_lo, int ph_hi) {
  extern __shared__ __attribute__((aligned(16))) char smem[];
  const u32 L0 = (u32)(size_t)(LAS char*)smem;
  const int tid = threadIdx.x, lane = tid & 63;
  const int w = __builtin_amdgcn_readfirstlane(tid >> 6);
  const int b = blockIdx.x, T = b & 7, s = b >> 3;
  const int l15 = lane & 15, g = lane >> 4, q4 = l15 >> 2, p4 = lane & 3;
  Sync sy; sy.flags = (GAS u64*)(ws + WS_FLAGS); sy.tag = 0;
  const bool multi = (NLAUNCH == 1);
  if (multi && w == 0) {
    const u64 e = __hip_atomic_load((GAS u64*)(ws + WS_EPOCH), __ATOMIC_RELAXED, __HIP_MEMORY_SCOPE_AGENT) + 1;
    sy.tag = ((u64)(u32)__builtin_amdgcn_readfirstlane((u32)(e >> 32)) << 32) | (u32)__builtin_amdgcn_readfirstlane((u32)e);
  }
  STAMP(0);
  const bool own35 = s < 30;
  const int rt3 = s / 10, ct3 = s - rt3 * 10;
  const int nrow3 = rt3 < 2 ? 96 : 64;
  const int row3 = 256 * T + 96 * rt3;

  auto bnarrow_voff = [&](int ktile8  ) -> u32 {
    const int k = 8 * ktile8 + (lane >> 3);
    const int sw = ((k >> 1) & 1) | (((k >> 3) & 1) << 1);
    const int seg = ((lane & 7) >> 1) ^ sw;
    return (u32)(k * (DM * 2) + ct3 * 128 + seg * 32 + (lane & 1) * 16);
  };

  if (ph_lo <= 1 && 1 < ph_hi) {
    if (s < 30) {
      const int hi15 = s >= 15, RT = 2 * T + hi15, ct = s - 15 * hi15;
      if (w >= 4) {
        const int lw = w - 4;
        const u32x4 rA = mk_rsrc(x, SEQ * DM * 2), rB = mk_rsrc(wqkv, DM * 3 * DM * 2);
        u32 vA[4], vB[4];
#pragma unroll
        for (int ii = 0; ii < 4; ++ii) {
          const int i = lw * 4 + ii;
          const int row = 8 * i + (lane >> 3), c = (lane & 7) ^ ((row >> 1) & 7);
          vA[ii] = (u32)((128 * RT + row) * 1280 + c * 16);
          const int k = 4 * i + g, sw = (k & 3) | (((k >> 3) & 1) << 2), seg = (l15 >> 1) ^ sw;
          vB[ii] = (u32)(k * 3840 + ct * 256 + seg * 32 + (lane & 1) * 16);
        }
        auto issue = [&](int c) {
          const u32 slot = L0 + (c & 3) * 32768 + lw * 4096;
#pragma unroll
          for (int ii = 0; ii < 4; ++ii) dma(rA, vA[ii], c * 128, slot + ii * 1024);
#pragma unroll
          for (int ii = 0; ii < 4; ++ii) dma(rB, vB[ii], c * (64 * 3840), slot + 16384 + ii * 1024);
        };
        issue(0); issue(1); issue(2);
        WAITV(16);
        const int t2 = tid - 256, srow = t2 >> 1, sh = t2 & 1;
        const u32 rs_addr = srow * 128 + sh * 64;
        const int rx = (srow >> 1) & 3;
        float s1 = 0.f, s2 = 0.f;
        f32x4 cs[2] = {{0, 0, 0, 0}, {0, 0, 0, 0}};
        const s16x8 ones = {0x3f80, 0x3f80, 0x3f80, 0x3f80, 0x3f80, 0x3f80, 0x3f80, 0x3f80};
        const int swz = q4 | ((g & 1) << 2);
        u32 wadr[2];
#pragma unroll
        for (int j = 0; j < 2; ++j) wadr[j] = 16384 + (8 * g + q4) * 256 + (((2 * lw + j) ^ swz) * 32) + 8 * p4;
        for (int c = 0; c < 10; ++c) {
          BAR();
          if (c + 3 < 10) issue(c + 3);
          const u32 slot = L0 + (c & 3) * 32768;
#pragma unroll
          for (int i = 0; i < 4; ++i) {
            const u32x4 v = __builtin_bit_cast(u32x4, lds_b128(slot + rs_addr + ((i ^ rx) * 16)));
#pragma unroll
            for (int e = 0; e < 4; ++e) { s1 = dot2(v[e], 0x3f803f80u, s1); s2 = dot2(v[e], v[e], s2); }
          }
#pragma unroll
          for (int ks = 0; ks < 2; ++ks)
#pragma unroll
            for (int j = 0; j < 2; ++j) {
              const u32 a = slot + wadr[j] + ks * 8192;
              cs[j] = mfma16(cat(lds_tr(a), lds_tr(a + 1024)), ones, cs[j]);
            }
          if (c + 3 < 10) WAITV(16); else if (c + 2 < 10) WAITV(8); else WAITV(0);
        }
        s1 += __shfl_xor(s1, 1); s2 += __shfl_xor(s2, 1);
        const float mean = s1 * (1.0f / DM), var = fmaxf(s2 * (1.0f / DM) - mean * mean, 0.f);
        float m, r; ln_stats(mean, var, m, r);
        if (sh == 0) *(LAS f32x2*)(size_t)(L0 + L_TAB + srow * 8) = (f32x2){m, r};
        if (l15 == 0) {
#pragma unroll
          for (int j = 0; j < 2; ++j) *(LAS f32x4*)(size_t)(L0 + L_TAB + 1024 + ((2 * lw + j) * 16 + 4 * g) * 4) = cs[j];
        }
        BAR();
        if (multi && own35) {
          const u32x4 rW = mk_rsrc(wo, DM * DM * 2);
          const u32 v0 = bnarrow_voff(0), v1 = bnarrow_voff(1);
          for (int i = lw * 20; i < lw * 20 + 20; i += 2) {
            dma(rW, v0, i * 8 * 1280, L0 + i * 1024);
            dma(rW, v1, i * 8 * 1280, L0 + i * 1024 + 1024);
          }
        }
      } else {
        const int wr = w >> 1, wc = w & 1;
        f32x4 acc[4][4];
#pragma unroll
        for (int i = 0; i < 4; ++i)
#pragma unroll
          for (int j = 0; j < 4; ++j) acc[i][j] = (f32x4){0, 0, 0, 0};
        u32 xadr[2], wadr[4];
        {
          const int row = 64 * wr + l15, f = (row >> 1) & 7;
          xadr[0] = row * 128 + ((g ^ f) * 16); xadr[1] = row * 128 + (((4 + g) ^ f) * 16);
          const int swz = q4 | ((g & 1) << 2);
#pragma unroll
          for (int j = 0; j < 4; ++j) wadr[j] = 16384 + (8 * g + q4) * 256 + (((4 * wc + j) ^ swz) * 32) + 8 * p4;
        }
        for (int c = 0; c < 10; ++c) {
          BAR();
          const u32 slot = L0 + (c & 3) * 32768;
#pragma unroll
          for (int ks = 0; ks < 2; ++ks) {
            s16x8 xf[4], wf[4];
#pragma unroll
            for (int i = 0; i < 4; ++i) xf[i] = lds_b128(slot + xadr[ks] + i * 2048);
#pragma unroll
            for (int j = 0; j < 4; ++j) { const u32 a = slot + wadr[j] + ks * 8192; wf[j] = cat(lds_tr(a), lds_tr(a + 1024)); }
#pragma unroll
            for (int i = 0; i < 4; ++i)
#pragma unroll
              for (int j = 0; j < 4; ++j) acc[i][j] = mfma16(wf[j], xf[i], acc[i][j]);
          }
        }
        BAR();
        const float qs = ct < 5 ? 0.22360679775f * 1.44269504089f : 1.0f;
        const __amdgpu_buffer_rsrc_t rO = mk_rs(ws + WS_QKV, SZ_QKV);
        f32x4 cs[4];
#pragma unroll
        for (int j = 0; j < 4; ++j) cs[j] = *(LAS f32x4*)(size_t)(L0 + L_TAB + 1024 + ((4 * wc + j) * 16 + 4 * g) * 4);
#pragma unroll
        for (int i = 0; i < 4; ++i) {
          const int row = 64 * wr + 16 * i + l15;
          const f32x2 st = *(LAS f32x2*)(size_t)(L0 + L_TAB + row * 8);
          const float rr = st[1] * qs;
#pragma unroll
          for (int j = 0; j < 4; ++j) {
            const f32x4 v = (acc[i][j] - cs[j] * st[0]) * rr;
            const u32x2 o = {pk_bf16(v[0], v[1]), pk_bf16(v[2], v[3])};
            __builtin_amdgcn_raw_buffer_store_b64(o, rO, (u32)((32 * ct + 16 * wc + 4 * j + g) * 16384 + (128 * RT + row) * 8), 0, 16);
          }
        }
        WAITV(0);
      }
      BAR();
      if (multi && tid == 0) sy.post(FL_QKV + RT * 15 + ct);
    } else {
      colsum_job(wg, wu, ws, 4 * (2 * T + s - 30), L0 + 96 * 1024, tid, lane, w);
    }
  }
  STAMP(1);

  if (ph_lo <= 2 && 2 < ph_hi) {
    constexpr u32 PL = 4128, LK = 81920, LV = LK + 5 * PL;
    *(LAS u32x2*)(size_t)(L0 + LV + 5 * PL + tid * 8) = (u32x2){0x00003f80u, 0u};
    if (multi && w == 0) {
      int idx = -1;
      if (lane < 24) {
        const int sec = lane >> 3, r4 = (lane >> 1) & 3, e = lane & 1;
        const int RT = 2 * T - 2 + r4;
        if (RT >= 0 && !(sec == 0 && r4 < 2)) idx = FL_QKV + RT * 15 + 5 * sec + (20 * s + 19 * e) / 128;
      }
      sy.wait(idx);
    }
    BAR();
    STAMP(2);
    {
      const u32x4 rQ = mk_rsrc(ws + WS_QKV, SZ_QKV);
      const u32 vq = lane * 16;
#pragma unroll
      for (int ii = 0; ii < 5; ++ii) {
        const int i = w * 5 + ii, tv = i >= 20, i2 = i - 20 * tv, j = i2 >> 2, qt = i2 & 3;
        if (T > 0 || qt >= 2)
          dma_c(rQ, vq, (u32)((160 + 160 * tv + 5 * s + j) * 16384 + (256 * (T - 1) + 128 * qt) * 8), L0 + (tv ? LV : LK) + j * PL + qt * 1024);
      }
    }
    const int q = lane & 31, h = lane >> 5;
    s16x8 q1; s16x4 q2;
    {
      const __amdgpu_buffer_rsrc_t rQ = mk_rs(ws + WS_QKV, SZ_QKV);
      const u32 tok = (u32)(256 * T + 32 * w + q) * 8;
      const u32x2 a = __builtin_amdgcn_raw_buffer_load_b64(rQ, (5 * s + 2 * h) * 16384 + tok, 0, 16);
      const u32x2 c = __builtin_amdgcn_raw_buffer_load_b64(rQ, (5 * s + 2 * h + 1) * 16384 + tok, 0, 16);
      const u32x2 d = __builtin_amdgcn_raw_buffer_load_b64(rQ, h ? OOB : (5 * s + 4) * 16384 + tok, 0, 16);
      q1 = cat(__builtin_bit_cast(s16x4, a), __builtin_bit_cast(s16x4, c));
      q2 = __builtin_bit_cast(s16x4, d);
    }
    WAITV(0);
    BAR();
    const int jstart = T > 0 ? 0 : 8 - w;
    const u32 kadr = L0 + LK + 2 * h * PL + (32 * w + q) * 8;
    const u32 k2adr = L0 + LK + 4 * PL + (32 * w + q) * 8;
    const u32 vadr = L0 + LV + (4 * (g & 1) + p4) * PL + (32 * w + 4 * h + q4) * 8;
    s16x8 k1[9]; s16x4 k2[9];
    const f32x16 zero16 = {0, 0, 0, 0, 0, 0, 0, 0, 0, 0, 0, 0, 0, 0, 0, 0};
    const int qd = q - 4 * h;
    float m = -3.0e38f;
    f32x16 sfar;
#pragma unroll
    for (int j = 0; j < 9; ++j) {
      if (j >= jstart) {
        k1[j] = cat(lds_b64(kadr + j * 256), lds_b64(kadr + PL + j * 256));
        k2[j] = lds_b64(k2adr + j * 256);
        f32x16 sc = mfma32k8(k2[j], q2, mfma32(k1[j], q1, zero16));
        if (j == 0) sfar = sc;
        else {
          if (j == 8) {
#pragma unroll
            for (int r = 0; r < 16; ++r) sc[r] = ((r & 3) + 8 * (r >> 2) <= qd) ? sc[r] : (T > 0 ? sfar[r] : -3.0e38f);
          }
#pragma unroll
          for (int r = 0; r < 16; r += 2) m = fmaxf(fmaxf(m, sc[r]), sc[r + 1]);
        }
      }
    }
    m = xhalf_max(m);
    f32x16 negm;
#pragma unroll
    for (int r = 0; r < 16; ++r) negm[r] = -m;
    f32x16 o = zero16;
#pragma unroll
    for (int j = 1; j < 8; ++j) {
      if (j >= jstart) {
        const f32x16 sc = mfma32k8(k2[j], q2, mfma32(k1[j], q1, negm));
        u32 pk[8];
#pragma unroll
        for (int i = 0; i < 8; ++i) pk[i] = pk_bf16(__builtin_amdgcn_exp2f(sc[2 * i]), __builtin_amdgcn_exp2f(sc[2 * i + 1]));
#pragma unroll
        for (int ks = 0; ks < 2; ++ks) {
          const u32 a = vadr + j * 256 + ks * 128;
          o = mfma32(cat(lds_tr(a), lds_tr(a + 64)), __builtin_bit_cast(s16x8, (u32x4){pk[4 * ks], pk[4 * ks + 1], pk[4 * ks + 2], pk[4 * ks + 3]}), o);
        }
      }
    }
    {
      f32x16 sd = mfma32k8(k2[8], q2, mfma32(k1[8], q1, negm));
      f32x16 sf;
      if (T > 0) sf = mfma32k8(k2[0], q2, mfma32(k1[0], q1, negm));
      u32 pd[8], pf[8];
#pragma unroll
      for (int i = 0; i < 8; ++i) {
        const bool v0 = ((2 * i) & 3) + 8 * ((2 * i) >> 2) <= qd, v1 = ((2 * i + 1) & 3) + 8 * ((2 * i + 1) >> 2) <= qd;
        const float e0 = v0 ? sd[2 * i] : (T > 0 ? sf[2 * i] : -3.0e38f), e1 = v1 ? sd[2 * i + 1] : (T > 0 ? sf[2 * i + 1] : -3.0e38f);
        const u32 pk = pk_bf16(__builtin_amdgcn_exp2f(e0), __builtin_amdgcn_exp2f(e1));
        const u32 mk = (v0 ? 0xffffu : 0u) | (v1 ? 0xffff0000u : 0u);
        pd[i] = pk & mk; pf[i] = pk & ~mk;
      }
#pragma unroll
      for (int ks = 0; ks < 2; ++ks) {
        const u32 a = vadr + 8 * 256 + ks * 128;
        o = mfma32(cat(lds_tr(a), lds_tr(a + 64)), __builtin_bit_cast(s16x8, (u32x4){pd[4 * ks], pd[4 * ks + 1], pd[4 * ks + 2], pd[4 * ks + 3]}), o);
      }
      if (T > 0) {
#pragma unroll
        for (int ks = 0; ks < 2; ++ks) {
          const u32 a = vadr + ks * 128;
          o = mfma32(cat(lds_tr(a), lds_tr(a + 64)), __builtin_bit_cast(s16x8, (u32x4){pf[4 * ks], pf[4 * ks + 1], pf[4 * ks + 2], pf[4 * ks + 3]}), o);
        }
      }
    }
    {
      auto r = __builtin_amdgcn_permlane32_swap(__float_as_uint(o[8]), __float_as_uint(o[8]), false, false);
      const float inv = 1.0f / __uint_as_float(r[1]);
      const __amdgpu_buffer_rsrc_t rO = mk_rs(ws + WS_O, SZ_O);
      const u32 tok = (u32)(256 * T + 32 * w + q) * 8;
#pragma unroll
      for (int i = 0; i < 3; ++i) {
        const u32x2 v = {pk_bf16(o[4 * i] * inv, o[4 * i + 1] * inv), pk_bf16(o[4 * i + 2] * inv, o[4 * i + 3] * inv)};
        __builtin_amdgcn_raw_buffer_store_b64(v, rO, (i == 2 && h) ? OOB : (u32)((5 * s + 2 * i + h) * 16384) + tok, 0, 16);
      }
    }
    WAITV(0);
    BAR();
    if (multi && tid == 0) sy.post(FL_ATT + T * 32 + s);
  }
  STAMP(3);

  u32 hres[3][2][2];
  const int wr3 = (w >> 1) & 1, wc3 = w & 1;
  u32 nb_adr[2];
  {
    const int sw = ((q4 >> 1) & 1) | ((g & 1) << 1);
#pragma unroll
    for (int j = 0; j < 2; ++j) nb_adr[j] = (8 * g + q4) * 128 + (((2 * wc3 + j) ^ sw) * 32) + 8 * p4;
  }

  if (ph_lo <= 3 && 3 < ph_hi) {
    if (own35) {
      constexpr u32 LA = 81920;
      u32x2 xr[3][2];
      if (w < 4) {
        const __amdgpu_buffer_rsrc_t rX = mk_rs(x, SEQ * DM * 2);
#pragma unroll
        for (int i = 0; i < 3; ++i)
#pragma unroll
          for (int j = 0; j < 2; ++j) {
            const int r = 48 * wr3 + 16 * i + l15;
            xr[i][j] = __builtin_amdgcn_raw_buffer_load_b64(rX, r < nrow3 ? (u32)((row3 + r) * 1280 + (64 * ct3 + 32 * wc3 + 16 * j + 4 * g) * 2) : OOB, 0, 0);
          }
      }
      if (!multi && w >= 4) {
        const int lw = w - 4;
        const u32x4 rW = mk_rsrc(wo, DM * DM * 2);
        const u32 v0 = bnarrow_voff(0), v1 = bnarrow_voff(1);
        for (int i = lw * 20; i < lw * 20 + 20; i += 2) { dma(rW, v0, i * 8 * 1280, L0 + i * 1024); dma(rW, v1, i * 8 * 1280, L0 + i * 1024 + 1024); }
      }
      if (multi && w == 0) sy.wait(lane < 32 ? FL_ATT + T * 32 + lane : -1);
      BAR();
      STAMP(4);
      if (w >= 4) {
        const int lw = w - 4;
        const u32x4 rO = mk_rsrc(ws + WS_O, SZ_O);
        u32 vA[3];
#pragma unroll
        for (int ii = 0; ii < 3; ++ii) {
          const int u = (lw * 3 + ii) * 64 + lane, pl = u / 48, un = (u - pl * 48) ^ (((pl >> 1) & 1) << 3);
          vA[ii] = 2 * un < nrow3 ? (u32)(pl * 16384 + (row3 + 2 * un) * 8) : OOB;
        }
        auto issue = [&](int c, int slot) {
#pragma unroll
          for (int ii = 0; ii < 3; ++ii) dma_c(rO, vA[ii], c * (16 * 16384), L0 + LA + slot * 12288 + (lw * 3 + ii) * 1024);
        };
        for (int c = 0; c < 5; ++c) issue(c, c);
        WAITV(12);
        int slot = 5;
        for (int c = 0; c < 10; ++c) {
          BAR();
          if (c + 5 < 10) { issue(c + 5, slot); slot = slot == 5 ? 0 : slot + 1; WAITV(12); }
          else if (c == 5) WAITV(9); else if (c == 6) WAITV(6); else if (c == 7) WAITV(3); else WAITV(0);
        }
        BAR();
      } else {
        f32x4 acc[3][2];
#pragma unroll
        for (int i = 0; i < 3; ++i) { acc[i][0] = (f32x4){0, 0, 0, 0}; acc[i][1] = (f32x4){0, 0, 0, 0}; }
        u32 aadr[3];
#pragma unroll
        for (int i = 0; i < 3; ++i) {
          const int tok = 48 * wr3 + 16 * i + l15;
          aadr[i] = (2 * g) * 768 + (((tok >> 1) ^ ((g & 1) << 3)) * 16) + (tok & 1) * 8;
        }
        int slot = 0;
        for (int c = 0; c < 10; ++c) {
          BAR();
          const u32 sa = L0 + LA + slot * 12288, sb = L0 + c * 8192;
          slot = slot == 5 ? 0 : slot + 1;
#pragma unroll
          for (int ks = 0; ks < 2; ++ks) {
            s16x8 xf[3], wf[2];
#pragma unroll
            for (int i = 0; i < 3; ++i) { const u32 a = sa + aadr[i] + ks * 8 * 768; xf[i] = cat(lds_b64(a), lds_b64(a + 768)); }
#pragma unroll
            for (int j = 0; j < 2; ++j) { const u32 a = sb + nb_adr[j] + ks * 4096; wf[j] = cat(lds_tr(a), lds_tr(a + 512)); }
#pragma unroll
            for (int i = 0; i < 3; ++i)
#pragma unroll
              for (int j = 0; j < 2; ++j) acc[i][j] = mfma16(wf[j], xf[i], acc[i][j]);
          }
        }
        BAR();
        const __amdgpu_buffer_rsrc_t rH = mk_rs(ws + WS_H, SZ_H);
#pragma unroll
        for (int i = 0; i < 3; ++i) {
          float hv[8];
#pragma unroll
          for (int j = 0; j < 2; ++j) {
            const u32 a0 = pk_bf16(acc[i][j][0], acc[i][j][1]), a1 = pk_bf16(acc[i][j][2], acc[i][j][3]);
            hres[i][j][0] = pk_bf16(bf_lo(xr[i][j][0]) + bf_lo(a0), bf_hi(xr[i][j][0]) + bf_hi(a0));
            hres[i][j][1] = pk_bf16(bf_lo(xr[i][j][1]) + bf_lo(a1), bf_hi(xr[i][j][1]) + bf_hi(a1));
            hv[4 * j] = bf_lo(hres[i][j][0]); hv[4 * j + 1] = bf_hi(hres[i][j][0]); hv[4 * j + 2] = bf_lo(hres[i][j][1]); hv[4 * j + 3] = bf_hi(hres[i][j][1]);
          }
          float sm = 0.f;
#pragma unroll
          for (int e = 0; e < 8; ++e) sm += hv[e];
          const float mw = sum_x16_x32(sm) * (1.0f / 32.0f);
          float m2 = 0.f;
#pragma unroll
          for (int e = 0; e < 8; ++e) { const float d = hv[e] - mw; m2 += d * d; }
          m2 = sum_x16_x32(m2);
          const int r = 48 * wr3 + 16 * i + l15;
          if (g == 0) *(LAS f32x2*)(size_t)(L0 + L_TAB + (r * 2 + wc3) * 8) = (f32x2){mw, m2};
          auto s0 = __builtin_amdgcn_permlane16_swap(hres[i][0][0], hres[i][1][0], false, false);
          auto s1 = __builtin_amdgcn_permlane16_swap(hres[i][0][1], hres[i][1][1], false, false);
          const int oct = 8 * ct3 + 4 * wc3 + ((g & 1) << 1) + (g >> 1);
          __builtin_amdgcn_raw_buffer_store_b128((u32x4){s0[0], s1[0], s0[1], s1[1]}, rH, r < nrow3 ? (u32)(oct * 32768 + (row3 + r) * 16) : OOB, 0, 16);
        }
      }
      BAR();
      if (tid < nrow3) {
        const f32x4 p = *(LAS f32x4*)(size_t)(L0 + L_TAB + tid * 16);
        const float mt = 0.5f * (p[0] + p[2]), d0 = p[0] - mt, d1 = p[2] - mt;
        const float m2 = p[1] + p[3] + 32.0f * (d0 * d0 + d1 * d1);
        __hip_atomic_store((GAS u64*)(ws + WS_HST) + (T * 10 + ct3) * 256 + 96 * rt3 + tid, ((u64)__float_as_uint(m2) << 32) | __float_as_uint(mt), __ATOMIC_RELAXED, __HIP_MEMORY_SCOPE_AGENT);
      }
      WAITV(0);
      BAR();
      if (multi && tid == 0) sy.post(FL_H + T * 30 + s);
    } else {
      colsum_job(wg, wu, ws, 4 * (2 * T + s - 30) + 2, L0 + 96 * 1024, tid, lane, w);
      if (multi && tid == 0) sy.post(FL_CS + 2 * T + s - 30);
    }
  }
  STAMP(5);

  if (ph_lo <= 4 && 4 < ph_hi) {
    if (s < 27) {
      const u32x4 rA = mk_rsrc(ws + WS_H, SZ_H), rG = mk_rsrc(wg, DM * FF * 2), rU = mk_rsrc(wu, DM * FF * 2);
      const u32 vN0 = [&] { const int k = 8 * w + (lane >> 3); const int sw = ((k >> 1) & 1) | (((k >> 3) & 1) << 1); return (u32)(k * (FF * 2) + s * 128 + ((((lane & 7) >> 1) ^ sw) * 32) + (lane & 1) * 16); }();
      auto issueB = [&](int c, int slot) {
        dma(rG, vN0, c * (64 * FF * 2), L0 + slot * 49152 + 32768 + w * 1024);
        dma(rU, vN0, c * (64 * FF * 2), L0 + slot * 49152 + 32768 + 8192 + w * 1024);
      };
      const u32 vA = (u32)((256 * T + lane) * 16);
      auto issueA = [&](int c, int slot) {
#pragma unroll
        for (int ii = 0; ii < 4; ++ii) dma_c(rA, vA, (8 * c + w) * 32768 + ii * 1024, L0 + slot * 49152 + w * 4096 + ii * 1024);
      };
      issueB(0, 0); issueB(1, 1);
      if (multi && w == 0) {
        int idx = -1;
        if (lane < 30) idx = FL_H + T * 30 + lane;
        else if (lane == 30) idx = FL_CS + (s >> 2);
        else if (lane == 31) idx = FL_CS + ((27 + s) >> 2);
        sy.wait(idx);
      }
      BAR();
      STAMP(6);
      {
        const __amdgpu_buffer_rsrc_t rS = mk_rs(ws + WS_HST, SZ_HST);
        const int row = tid >> 1, hf = tid & 1;
        u32x2 pp[5];
#pragma unroll
        for (int i = 0; i < 5; ++i) pp[i] = __builtin_amdgcn_raw_buffer_load_b64(rS, (u32)(((T * 10 + 5 * hf + i) * 256 + row) * 8), 0, 16);
        float sm = 0.f;
#pragma unroll
        for (int i = 0; i < 5; ++i) sm += __uint_as_float(pp[i][0]);
        sm += __shfl_xor(sm, 1);
        const float mean = sm * 0.1f;
        float m2 = 0.f;
#pragma unroll
        for (int i = 0; i < 5; ++i) { const float d = __uint_as_float(pp[i][0]) - mean; m2 += __uint_as_float(pp[i][1]) + 64.0f * d * d; }
        m2 += __shfl_xor(m2, 1);
        float m, r; ln_stats(mean, m2 * (1.0f / DM), m, r);
        if (hf == 0) *(LAS f32x2*)(size_t)(L0 + L_TAB + row * 8) = (f32x2){m, r};
        if (tid < 128) {
          const int G = (tid >> 6) * 27 + s;
          *(LAS float*)(size_t)(L0 + L_TAB + 2048 + tid * 4) = __hip_atomic_load((GAS float*)(ws + WS_CS) + G * 64 + (tid & 63), __ATOMIC_RELAXED, __HIP_MEMORY_SCOPE_AGENT);
        }
      }
      issueA(0, 0); issueA(1, 1);
      WAITV(4);
      const int wr = w >> 1, wc = w & 1;
      f32x4 ag[4][2], au[4][2];
#pragma unroll
      for (int i = 0; i < 4; ++i)
#pragma unroll
        for (int j = 0; j < 2; ++j) { ag[i][j] = (f32x4){0, 0, 0, 0}; au[i][j] = (f32x4){0, 0, 0, 0}; }
      const u32 xadr = g * 4096 + (64 * wr + l15) * 16;
      u32 wadr[2];
      {
        const int sw = ((q4 >> 1) & 1) | ((g & 1) << 1);
#pragma unroll
        for (int j = 0; j < 2; ++j) wadr[j] = 32768 + (8 * g + q4) * 128 + (((2 * wc + j) ^ sw) * 32) + 8 * p4;
      }
      int slot = 0, nslot = 2;
      for (int c = 0; c < 10; ++c) {
        BAR();
        if (c + 2 < 10) { issueA(c + 2, nslot); issueB(c + 2, nslot); nslot = nslot == 2 ? 0 : nslot + 1; }
        const u32 sb = L0 + slot * 49152; slot = slot == 2 ? 0 : slot + 1;
#pragma unroll
        for (int ks = 0; ks < 2; ++ks) {
          s16x8 xf[4], gf[2], uf[2];
#pragma unroll
          for (int i = 0; i < 4; ++i) xf[i] = lds_b128(sb + xadr + ks * 16384 + i * 256);
#pragma unroll
          for (int j = 0; j < 2; ++j) {
            const u32 a = sb + wadr[j] + ks * 4096;
            gf[j] = cat(lds_tr(a), lds_tr(a + 512)); uf[j] = cat(lds_tr(a + 8192), lds_tr(a + 8192 + 512));
          }
#pragma unroll
          for (int i = 0; i < 4; ++i)
#pragma unroll
            for (int j = 0; j < 2; ++j) { ag[i][j] = mfma16(gf[j], xf[i], ag[i][j]); au[i][j] = mfma16(uf[j], xf[i], au[i][j]); }
        }
        if (c + 2 < 10) WAITV(6); else WAITV(0);
      }
      BAR();
      {
        const __amdgpu_buffer_rsrc_t rH = mk_rs(ws + WS_G, SZ_G);
        f32x4 cg[2], cu[2];
#pragma unroll
        for (int j = 0; j < 2; ++j) {
          cg[j] = *(LAS f32x4*)(size_t)(L0 + L_TAB + 2048 + (32 * wc + 16 * j + 4 * g) * 4);
          cu[j] = *(LAS f32x4*)(size_t)(L0 + L_TAB + 2048 + 256 + (32 * wc + 16 * j + 4 * g) * 4);
        }
#pragma unroll
        for (int i = 0; i < 4; ++i) {
          const int row = 64 * wr + 16 * i + l15;
          const f32x2 st = *(LAS f32x2*)(size_t)(L0 + L_TAB + row * 8);
          const float r2 = st[1] * st[1];
          u32 pk[2][2];
#pragma unroll
          for (int j = 0; j < 2; ++j) {
            f32x4 a = ag[i][j] - cg[j] * st[0], u = (au[i][j] - cu[j] * st[0]) * r2;
#pragma unroll
            for (int e = 0; e < 4; ++e) a[e] = fmaxf(a[e], 0.f) * u[e];
            pk[j][0] = pk_bf16(a[0], a[1]); pk[j][1] = pk_bf16(a[2], a[3]);
          }
          auto s0 = __builtin_amdgcn_permlane16_swap(pk[0][0], pk[1][0], false, false);
          auto s1 = __builtin_amdgcn_permlane16_swap(pk[0][1], pk[1][1], false, false);
          const int oct = 8 * s + 4 * wc + ((g & 1) << 1) + (g >> 1);
          __builtin_amdgcn_raw_buffer_store_b128((u32x4){s0[0], s1[0], s0[1], s1[1]}, rH, (u32)(oct * 32768 + (256 * T + row) * 16), 0, 16);
        }
      }
      WAITV(0);
      BAR();
      if (multi && tid == 0) sy.post(FL_FF + T * 27 + s);
    }
  }
  STAMP(7);

  if (ph_lo <= 5 && 5 < ph_hi) {
    if (own35) {
      constexpr u32 LA = 98304;
      const u32x4 rW = mk_rsrc(wd, FF * DM * 2);
      const int lw = w - 4;
      const u32 vB0 = bnarrow_voff(2 * (lw & 3)), vB1 = bnarrow_voff(2 * (lw & 3) + 1);
      auto issueB = [&](int c, int slot) {
        dma(rW, vB0, c * (64 * 1280), L0 + slot * 8192 + lw * 2048);
        dma(rW, vB1, c * (64 * 1280), L0 + slot * 8192 + lw * 2048 + 1024);
      };
      if (w >= 4) for (int c = 0; c < 12; ++c) issueB(c, c);
      if (!multi && w < 4) {
        const __amdgpu_buffer_rsrc_t rH = mk_rs(ws + WS_H, SZ_H);
#pragma unroll
        for (int i = 0; i < 3; ++i)
#pragma unroll
          for (int j = 0; j < 2; ++j) {
            const int r = 48 * wr3 + 16 * i + l15, col = 64 * ct3 + 32 * wc3 + 16 * j + 4 * g;
            const u32x2 v = __builtin_amdgcn_raw_buffer_load_b64(rH, r < nrow3 ? (u32)((col >> 3) * 32768 + (row3 + r) * 16 + (col & 7) * 2) : OOB, 0, 0);
            hres[i][j][0] = v[0]; hres[i][j][1] = v[1];
          }
      }
      if (multi && w == 0) sy.wait(lane < 27 ? FL_FF + T * 27 + lane : -1);
      BAR();
      STAMP(8);
      if (w >= 4) {
        const u32x4 rG = mk_rsrc(ws + WS_G, SZ_G);
        u32 vA[3];
#pragma unroll
        for (int ii = 0; ii < 3; ++ii) {
          const int u = (lw * 3 + ii) * 64 + lane, pl = u / 96, tk = u - pl * 96;
          vA[ii] = tk < nrow3 ? (u32)(pl * 32768 + (row3 + tk) * 16) : OOB;
        }
        auto issueA = [&](int c, int slot) {
#pragma unroll
          for (int ii = 0; ii < 3; ++ii) dma_c(rG, vA[ii], c * (8 * 32768), L0 + LA + slot * 12288 + (lw * 3 + ii) * 1024);
        };
        for (int c = 0; c < 4; ++c) issueA(c, c);
        WAITV(9);
        int sa = 4, sb = 0;
        for (int c = 0; c < 27; ++c) {
          BAR();
          if (c + 4 < 27) { issueA(c + 4, sa); sa = sa == 4 ? 0 : sa + 1; }
          if (c >= 1 && c + 11 < 27) { issueB(c + 11, sb); sb = sb == 11 ? 0 : sb + 1; }
          if (c >= 4 && c <= 15) WAITV(17);
          else if (c <= 22) WAITV(9);
          else if (c == 23) WAITV(6);
          else if (c == 24) WAITV(3);
          else WAITV(0);
        }
        BAR();
      } else {
        f32x4 acc[3][2];
#pragma unroll
        for (int i = 0; i < 3; ++i) { acc[i][0] = (f32x4){0, 0, 0, 0}; acc[i][1] = (f32x4){0, 0, 0, 0}; }
        const u32 aadr = g * 1536 + (48 * wr3 + l15) * 16;
        int sa = 0, sb = 0;
        for (int c = 0; c < 27; ++c) {
          BAR();
          const u32 pa = L0 + LA + sa * 12288, pb = L0 + sb * 8192;
          sa = sa == 4 ? 0 : sa + 1; sb = sb == 11 ? 0 : sb + 1;
#pragma unroll
          for (int ks = 0; ks < 2; ++ks) {
            s16x8 xf[3], wf[2];
#pragma unroll
            for (int i = 0; i < 3; ++i) xf[i] = lds_b128(pa + aadr + ks * 4 * 1536 + i * 256);
#pragma unroll
            for (int j = 0; j < 2; ++j) { const u32 a = pb + nb_adr[j] + ks * 4096; wf[j] = cat(lds_tr(a), lds_tr(a + 512)); }
#pragma unroll
            for (int i = 0; i < 3; ++i)
#pragma unroll
              for (int j = 0; j < 2; ++j) acc[i][j] = mfma16(wf[j], xf[i], acc[i][j]);
          }
        }
        BAR();
#pragma unroll
        for (int i = 0; i < 3; ++i) {
          u32 pk[2][2];
#pragma unroll
          for (int j = 0; j < 2; ++j) {
            const u32 a0 = pk_bf16(acc[i][j][0], acc[i][j][1]), a1 = pk_bf16(acc[i][j][2], acc[i][j][3]);
            pk[j][0] = pk_bf16(bf_lo(hres[i][j][0]) + bf_lo(a0), bf_hi(hres[i][j][0]) + bf_hi(a0));
            pk[j][1] = pk_bf16(bf_lo(hres[i][j][1]) + bf_lo(a1), bf_hi(hres[i][j][1]) + bf_hi(a1));
          }
          auto s0 = __builtin_amdgcn_permlane16_swap(pk[0][0], pk[1][0], false, false);
          auto s1 = __builtin_amdgcn_permlane16_swap(pk[0][1], pk[1][1], false, false);
          const int r = 48 * wr3 + 16 * i + l15;
          const int oct = 8 * ct3 + 4 * wc3 + ((g & 1) << 1) + (g >> 1);
          if (r < nrow3) *(u32x4*)(out + (size_t)(row3 + r) * DM + oct * 8) = (u32x4){s0[0], s1[0], s0[1], s1[1]};
        }
      }
    }
    if (multi && b == 0 && w == 0) {
#pragma unroll
      for (int i = 0; i < 4; ++i) sy.wait(FL_ATT + i * 64 + lane);
      if (lane == 0) __hip_atomic_store((GAS u64*)(ws + WS_EPOCH), sy.tag, __ATOMIC_RELAXED, __HIP_MEMORY_SCOPE_AGENT);
    }
  }
  STAMP(9);
}

extern "C" void kernel_launch(void* const* d_in, const int* in_sizes, int n_in, void* d_out, int out_size, void* d_ws, size_t ws_size, hipStream_t stream) {
  (void)in_sizes; (void)n_in; (void)out_size; (void)ws_size;
  const u16* x = (const u16*)d_in[0]; const u16* wqkv = (const u16*)d_in[1]; const u16* wo = (const u16*)d_in[2];
  const u16* wg = (const u16*)d_in[4]; const u16* wu = (const u16*)d_in[5]; const u16* wd = (const u16*)d_in[6];
  (void)hipFuncSetAttribute((const void*)fused_block, hipFuncAttributeMaxDynamicSharedMemorySize, LDS_BYTES);
  fused_block<<<256, 512, LDS_BYTES, stream>>>(x, wqkv, wo, wg, wu, wd, (u16*)d_out, (char*)d_ws, 1, 6);
}
```

```cpp
#include <hip/hip_runtime.h>
#include <stdint.h>

typedef unsigned short u16;
typedef unsigned int u32;
typedef unsigned long long u64;
typedef short s16x4 __attribute__((ext_vector_type(4)));
typedef short s16x8 __attribute__((ext_vector_type(8)));
typedef __bf16 bf16x8 __attribute__((ext_vector_type(8)));
typedef __bf16 bf16x2 __attribute__((ext_vector_type(2)));
typedef float f32x2 __attribute__((ext_vector_type(2)));
typedef float f32x4 __attribute__((ext_vector_type(4)));
typedef float f32x16 __attribute__((ext_vector_type(16)));
typedef u32 u32x2 __attribute__((ext_vector_type(2)));
typedef u32 u32x4 __attribute__((ext_vector_type(4)));
#define LAS __attribute__((address_space(3)))
#define GAS __attribute__((address_space(1)))
#define DEV __device__ __forceinline__

#define NLAUNCH 1
#define STAMPS 0

constexpr int SEQ = 2048, DM = 640, FF = 1728;
constexpr u32 WS_EPOCH = 0;
constexpr u32 WS_FLAGS = 4096;
constexpr u32 FL_QKV = 0, FL_ATT = 256, FL_H = 512, FL_FF = 768, FL_CS = 1000;
constexpr u32 WS_QKV = 256 * 1024;
constexpr u32 SZ_QKV = 480 * 16384;
constexpr u32 WS_O = WS_QKV + SZ_QKV;
constexpr u32 SZ_O = 160 * 16384;
constexpr u32 WS_H = WS_O + SZ_O;
constexpr u32 SZ_H = 80 * 32768;
constexpr u32 WS_HST = WS_H + SZ_H;
constexpr u32 SZ_HST = 8 * 10 * 256 * 8;
constexpr u32 WS_G = WS_HST + SZ_HST;
constexpr u32 SZ_G = 216 * 32768;
constexpr u32 WS_CS = WS_G + SZ_G;
constexpr u32 SZ_CS = 54 * 64 * 4;
constexpr u32 WS_STAMP = WS_CS + 16384;
constexpr u32 LDS_BYTES = 160 * 1024;
constexpr u32 L_TAB = 156 * 1024;
constexpr int NTHR = 768;
constexpr u32 OOB = 0x80000000u;

DEV u32x4 mk_rsrc(const void* p, u32 bytes) {
  const u64 a = (u64)p;
  u32x4 r; r[0] = (u32)a; r[1] = (u32)(a >> 32) & 0xffffu; r[2] = bytes; r[3] = 0x00020000u; return r;
}
DEV __amdgpu_buffer_rsrc_t mk_rs(const void* p, u32 bytes) { return __builtin_amdgcn_make_buffer_rsrc((void*)p, 0, bytes, 0x00020000); }
DEV void dma(u32x4 rs, u32 voff, u32 soff, u32 lds) {
  asm volatile("s_mov_b32 m0, %0\n\ts_nop 0\n\tbuffer_load_dwordx4 %1, %2, %3 offen lds" ::"s"(lds), "v"(voff), "s"(rs), "s"(soff));
}
DEV void dma_c(u32x4 rs, u32 voff, u32 soff, u32 lds) {
  asm volatile("s_mov_b32 m0, %0\n\ts_nop 0\n\tbuffer_load_dwordx4 %1, %2, %3 offen sc1 lds" ::"s"(lds), "v"(voff), "s"(rs), "s"(soff));
}
#define WAITV(n) asm volatile("s_waitcnt vmcnt(" #n ")" ::: "memory")
#define BAR() asm volatile("s_waitcnt lgkmcnt(0)\n\ts_barrier" ::: "memory")
DEV u32 pk_bf16(float lo, float hi) { f32x2 v = {lo, hi}; return __builtin_bit_cast(u32, __builtin_convertvector(v, bf16x2)); }
DEV float bf_lo(u32 d) { return __uint_as_float(d << 16); }
DEV float bf_hi(u32 d) { return __uint_as_float(d & 0xffff0000u); }
DEV float rbf(float x) { return bf_lo(pk_bf16(x, x)); }
DEV s16x8 lds_b128(u32 a) { return *(LAS s16x8*)(size_t)a; }
DEV s16x4 lds_b64(u32 a) { return *(LAS s16x4*)(size_t)a; }
DEV s16x4 lds_tr(u32 a) { return __builtin_amdgcn_ds_read_tr16_b64_v4i16((LAS s16x4*)(size_t)a); }
DEV s16x8 cat(s16x4 a, s16x4 b) { return __builtin_shufflevector(a, b, 0, 1, 2, 3, 4, 5, 6, 7); }
DEV f32x4 mfma16(s16x8 a, s16x8 b, f32x4 c) { return __builtin_amdgcn_mfma_f32_16x16x32_bf16(__builtin_bit_cast(bf16x8, a), __builtin_bit_cast(bf16x8, b), c, 0, 0, 0); }
DEV f32x16 mfma32(s16x8 a, s16x8 b, f32x16 c) { return __builtin_amdgcn_mfma_f32_32x32x16_bf16(__builtin_bit_cast(bf16x8, a), __builtin_bit_cast(bf16x8, b), c, 0, 0, 0); }
DEV f32x16 mfma32k8(s16x4 a, s16x4 b, f32x16 c) { return __builtin_amdgcn_mfma_f32_32x32x8bf16_1k(a, b, c, 0, 0, 0); }
DEV float dot2(u32 a, u32 b, float c) { return __builtin_amdgcn_fdot2_f32_bf16(__builtin_bit_cast(bf16x2, a), __builtin_bit_cast(bf16x2, b), c, false); }
DEV float xhalf_max(float m) { auto r = __builtin_amdgcn_permlane32_swap(__float_as_uint(m), __float_as_uint(m), false, false); return fmaxf(__uint_as_float(r[0]), __uint_as_float(r[1])); }
DEV float sum_x16_x32(float s) { s += __shfl_xor(s, 16); s += __shfl_xor(s, 32); return s; }
DEV void ln_stats(float mean, float var, float& m_out, float& r_out) {
  m_out = rbf(mean);
  const float sd = rbf(__builtin_sqrtf(rbf(rbf(var) + 1e-5f)));
  r_out = 1.0f / sd;
}

struct Sync {
  GAS u64* flags; u64 tag;
  DEV void wait(int idx) const {
    GAS u64* p = flags + (idx < 0 ? 0 : idx) * 16;
    for (u32 spins = 0; spins < (1u << 16); ++spins) {
      const u64 v = __hip_atomic_load(p, __ATOMIC_RELAXED, __HIP_MEMORY_SCOPE_AGENT);
      if (__all(idx < 0 || v == tag)) break;
      __builtin_amdgcn_s_sleep(1);
    }
  }
  DEV void post(int idx) const { __hip_atomic_store(flags + idx * 16, tag, __ATOMIC_RELAXED, __HIP_MEMORY_SCOPE_AGENT); }
};

#define CSTAMP(i) do {} while (0)
#define STAMP(i) do {} while (0)
#define STAMPW(i, wv) do {} while (0)

DEV void colsum_job(const u16* wg, const u16* wu, char* ws, int G0, u32 lds0, int tid, int lane, int w) {
  if (w < 8) {
  u32x4 v[2][10];
#pragma unroll
  for (int sg = 0; sg < 2; ++sg) {
    int G = G0 + sg; if (G > 53) G = 53;
    const u16* W = (G < 27 ? wg + G * 64 : wu + (G - 27) * 64) + (lane & 7) * 8 + (size_t)(8 * w + (lane >> 3)) * FF;
#pragma unroll
    for (int i = 0; i < 10; ++i) v[sg][i] = *(const u32x4*)(W + (size_t)i * 64 * FF);
  }
#pragma unroll
  for (int sg = 0; sg < 2; ++sg) {
    float a[8];
#pragma unroll
    for (int e = 0; e < 8; ++e) a[e] = 0.f;
#pragma unroll
    for (int i = 0; i < 10; ++i)
#pragma unroll
      for (int e = 0; e < 4; ++e) { a[2 * e] += bf_lo(v[sg][i][e]); a[2 * e + 1] += bf_hi(v[sg][i][e]); }
#pragma unroll
    for (int e = 0; e < 8; ++e) { float s = a[e]; s += __shfl_xor(s, 8); s = sum_x16_x32(s); a[e] = s; }
    if (lane < 8) {
      LAS f32x4* p = (LAS f32x4*)(size_t)(lds0 + ((sg * 8 + w) * 64 + lane * 8) * 4);
      p[0] = (f32x4){a[0], a[1], a[2], a[3]}; p[1] = (f32x4){a[4], a[5], a[6], a[7]};
    }
  }
  }
  BAR();
  if (tid < 128) {
    const int sg = tid >> 6, c = tid & 63; float s = 0.f;
#pragma unroll
    for (int ww = 0; ww < 8; ++ww) s += *(LAS float*)(size_t)(lds0 + ((sg * 8 + ww) * 64 + c) * 4);
    if (G0 + sg < 54) __hip_atomic_store((GAS float*)(ws + WS_CS) + (G0 + sg) * 64 + c, s, __ATOMIC_RELAXED, __HIP_MEMORY_SCOPE_AGENT);
  }
  WAITV(0);
  BAR();
}

#define SCHED0() __builtin_amdgcn_sched_barrier(0)
#define SGB(mask, n) __builtin_amdgcn_sched_group_barrier(mask, n, 0)
constexpr int M_MFMA = 0x8, M_DSR = 0x100, M_VALU = 0x2;

__global__ __launch_bounds__(768, 3) void fused_block(const u16* __restrict__ x, const u16* __restrict__ wqkv, const u16* __restrict__ wo,
                                                      const u16* __restrict__ wg, const u16* __restrict__ wu, const u16* __restrict__ wd,
                                                      u16* __restrict__ out, char* __restrict__ ws, int ph_lo, int ph_hi) {
  extern __shared__ __attribute__((aligned(16))) char smem[];
  const u32 L0 = (u32)(size_t)(LAS char*)smem;
  const int tid = threadIdx.x, lane = tid & 63;
  const int w = __builtin_amdgcn_readfirstlane(tid >> 6);
  const int lw = w - 8;
  const int b = blockIdx.x, T = b & 7, s = b >> 3;
  const int l15 = lane & 15, g = lane >> 4, q4 = l15 >> 2, p4 = lane & 3;
  Sync sy; sy.flags = (GAS u64*)(ws + WS_FLAGS); sy.tag = 0;
  const bool multi = (NLAUNCH == 1);
  if (multi && w == 0) {
    const u64 e = __hip_atomic_load((GAS u64*)(ws + WS_EPOCH), __ATOMIC_RELAXED, __HIP_MEMORY_SCOPE_AGENT) + 1;
    sy.tag = ((u64)(u32)__builtin_amdgcn_readfirstlane((u32)(e >> 32)) << 32) | (u32)__builtin_amdgcn_readfirstlane((u32)e);
  }
  STAMP(0);
  const bool own35 = s < 30;
  const int rt3 = s / 10, ct3 = s - rt3 * 10;
  const int nrow3 = rt3 < 2 ? 96 : 64;
  const int row3 = 256 * T + 96 * rt3;

  auto bn_voff = [&](int i8, int pitch, int colb) -> u32 {
    const int k = 8 * i8 + (lane >> 3);
    const int sw = ((k >> 1) & 1) | (((k >> 3) & 1) << 1);
    return (u32)(k * pitch + colb + ((((lane & 7) >> 1) ^ sw) * 32) + (lane & 1) * 16);
  };
  auto prefetch_wo = [&]() {
    const u32x4 rW = mk_rsrc(wo, DM * DM * 2);
    const u32 v0 = bn_voff(0, 1280, ct3 * 128), v1 = bn_voff(1, 1280, ct3 * 128);
#pragma unroll 1
    for (int i = lw * 20; i < lw * 20 + 20; i += 2) {
      dma(rW, v0, i * 8 * 1280, L0 + i * 1024);
      dma(rW, v1, i * 8 * 1280, L0 + i * 1024 + 1024);
    }
  };

  if (ph_lo <= 1 && 1 < ph_hi) {
    if (s < 30) {
      const int hi15 = s >= 15, RT = 2 * T + hi15, ct = s - 15 * hi15;
      constexpr u32 TAB = 120 * 1024;
      if (w >= 8) {
        const u32x4 rA = mk_rsrc(x, SEQ * DM * 2), rB = mk_rsrc(wqkv, DM * 3 * DM * 2);
        u32 vA[4], vB[4];
#pragma unroll
        for (int ii = 0; ii < 4; ++ii) {
          const int i = lw * 4 + ii;
          const int row = 8 * i + (lane >> 3), c = (lane & 7) ^ ((row >> 1) & 7);
          vA[ii] = (u32)((128 * RT + row) * 1280 + c * 16);
          const int k = 4 * i + g, sw = (k & 3) | (((k >> 3) & 1) << 2), seg = (l15 >> 1) ^ sw;
          vB[ii] = (u32)(k * 3840 + ct * 256 + seg * 32 + (lane & 1) * 16);
        }
        auto issue = [&](int c, int slot) {
          const u32 base = L0 + slot * 32768 + lw * 4096;
#pragma unroll
          for (int ii = 0; ii < 4; ++ii) dma(rA, vA[ii], c * 128, base + ii * 1024);
#pragma unroll
          for (int ii = 0; ii < 4; ++ii) dma(rB, vB[ii], c * (64 * 3840), base + 16384 + ii * 1024);
        };
#pragma unroll 1
        for (int c = 0; c < 4; ++c) issue(c, c);
        STAMPW(10, 8);
        WAITV(24); BAR();
        STAMPW(11, 8);
        WAITV(16);
        int slot = 4;
#pragma unroll 1
        for (int c = 0; c < 10; ++c) {
          BAR();
          if (c < 6) { issue(c + 4, slot); slot = slot == 4 ? 0 : slot + 1; WAITV(16); }
          else if (c == 6) WAITV(8); else WAITV(0);
        }
        BAR();
      } else {
        const int wr = w >> 2, wc = w & 3;
        f32x4 acc[4][2], cs = {0, 0, 0, 0};
#pragma unroll
        for (int i = 0; i < 4; ++i) { acc[i][0] = (f32x4){0, 0, 0, 0}; acc[i][1] = (f32x4){0, 0, 0, 0}; }
        f32x4 s1 = {0, 0, 0, 0}, s2 = {0, 0, 0, 0};
        const s16x8 ones = {0x3f80, 0x3f80, 0x3f80, 0x3f80, 0x3f80, 0x3f80, 0x3f80, 0x3f80};
        u32 xa[4][2], wa[2];
        {
          const int f = (l15 >> 1) & 7, swz = q4 | ((g & 1) << 2);
#pragma unroll
          for (int i = 0; i < 4; ++i) {
            const int row = 64 * wr + 16 * ((i + wc) & 3) + l15;
            xa[i][0] = row * 128 + ((g ^ f) * 16); xa[i][1] = row * 128 + (((4 + g) ^ f) * 16);
          }
#pragma unroll
          for (int j = 0; j < 2; ++j) wa[j] = 16384 + (8 * g + q4) * 256 + (((2 * wc + (j ^ wr)) ^ swz) * 32) + 8 * p4;
        }
        s16x8 xA[4], wA[2], xB[4], wB[2];
#define P1_READ(X, W, base, ks) do { _Pragma("unroll") for (int i = 0; i < 4; ++i) X[i] = lds_b128((base) + xa[i][ks]); \
          _Pragma("unroll") for (int j = 0; j < 2; ++j) { const u32 a_ = (base) + wa[j] + (ks) * 8192; W[j] = cat(lds_tr(a_), lds_tr(a_ + 1024)); } } while (0)
#define P1_MMA(X, W) do { _Pragma("unroll") for (int i = 0; i < 4; ++i) _Pragma("unroll") for (int j = 0; j < 2; ++j) acc[i][j] = mfma16(W[j], X[i], acc[i][j]); \
          cs = mfma16(W[0], ones, cs); s1 = mfma16(ones, X[0], s1); s2 = mfma16(X[0], X[0], s2); } while (0)
#define P1_SCHED() do { _Pragma("unroll") for (int z = 0; z < 4; ++z) { SGB(M_DSR, 2); SGB(M_MFMA, 2); } SGB(M_MFMA, 3); SCHED0(); } while (0)
        BAR();
        CSTAMP(28);
        P1_READ(xA, wA, L0, 0);
        u32 base = L0;
#pragma unroll 1
        for (int c = 0; c < 10; ++c) {
          BAR(); SCHED0();
          P1_READ(xB, wB, base, 1);
          P1_MMA(xA, wA);
          P1_SCHED();
          base = base == L0 + 4 * 32768 ? L0 : base + 32768;
          P1_READ(xA, wA, base, 0);
          P1_MMA(xB, wB);
          P1_SCHED();
        }
        STAMP(12);
        CSTAMP(29);
        {
          const float sq = (l15 & 2) ? ((l15 & 1) ? s2[3] : s2[2]) : ((l15 & 1) ? s2[1] : s2[0]);
          const float mean = s1[0] * (1.0f / DM), var = fmaxf(sq * (1.0f / DM) - mean * mean, 0.f);
          float m, r; ln_stats(mean, var, m, r);
          if (g == q4) *(LAS f32x2*)(size_t)(L0 + TAB + (64 * wr + 16 * wc + l15) * 8) = (f32x2){m, r};
          if (l15 == 0) *(LAS f32x4*)(size_t)(L0 + TAB + 1024 + ((2 * wc + wr) * 16 + 4 * g) * 4) = cs;
        }
        BAR();
        STAMP(13);
        const float qs = ct < 5 ? 0.22360679775f * 1.44269504089f : 1.0f;
        const __amdgpu_buffer_rsrc_t rO = mk_rs(ws + WS_QKV, SZ_QKV);
        f32x4 cj[2];
#pragma unroll
        for (int j = 0; j < 2; ++j) cj[j] = *(LAS f32x4*)(size_t)(L0 + TAB + 1024 + ((2 * wc + (j ^ wr)) * 16 + 4 * g) * 4);
#pragma unroll
        for (int i = 0; i < 4; ++i) {
          const int row = 64 * wr + 16 * ((i + wc) & 3) + l15;
          const f32x2 st = *(LAS f32x2*)(size_t)(L0 + TAB + row * 8);
          const float rr = st[1] * qs;
#pragma unroll
          for (int j = 0; j < 2; ++j) {
            const f32x4 v = (acc[i][j] - cj[j] * st[0]) * rr;
            const u32x2 o = {pk_bf16(v[0], v[1]), pk_bf16(v[2], v[3])};
            __builtin_amdgcn_raw_buffer_store_b64(o, rO, (u32)((32 * ct + 4 * (2 * wc + (j ^ wr)) + g) * 16384 + (128 * RT + row) * 8), 0, 16);
          }
        }
        STAMP(14);
        WAITV(0);
        STAMP(15);
      }
      BAR();
      if (multi && tid == 0) sy.post(FL_QKV + RT * 15 + ct);
    } else {
      colsum_job(wg, wu, ws, 4 * (2 * T + s - 30), L0 + 96 * 1024, tid, lane, w);
    }
  }
  STAMP(1);

  if (ph_lo <= 2 && 2 < ph_hi) {
    constexpr u32 PL = 4128, LK = 81920, LV = LK + 5 * PL;
    if (tid < 512) *(LAS u32x2*)(size_t)(L0 + LV + 5 * PL + tid * 8) = (u32x2){0x00003f80u, 0u};
    if (multi && w == 0) {
      int idx = -1;
      if (lane < 24) {
        const int sec = lane >> 3, r4 = (lane >> 1) & 3, e = lane & 1;
        const int RT = 2 * T - 2 + r4;
        if (RT >= 0 && !(sec == 0 && r4 < 2)) idx = FL_QKV + RT * 15 + 5 * sec + (20 * s + 19 * e) / 128;
      }
      sy.wait(idx);
    }
    BAR();
    STAMP(2);
    if (w >= 8) {
      const u32x4 rQ = mk_rsrc(ws + WS_QKV, SZ_QKV);
      if (T > 0 || lw >= 2) {
#pragma unroll 1
        for (int i = 0; i < 10; ++i) {
          const int tv = i >= 5, j = i - 5 * tv;
          dma_c(rQ, lane * 16, (u32)((160 + 160 * tv + 5 * s + j) * 16384 + (256 * (T - 1) + 128 * lw) * 8), L0 + (tv ? LV : LK) + j * PL + lw * 1024);
        }
      }
      WAITV(0);
      BAR();
      if (multi && own35) prefetch_wo();
    } else {
      const int q = lane & 31, h = lane >> 5;
      s16x8 q1; s16x4 q2;
      {
        const __amdgpu_buffer_rsrc_t rQ = mk_rs(ws + WS_QKV, SZ_QKV);
        const u32 tok = (u32)(256 * T + 32 * w + q) * 8;
        const u32x2 a = __builtin_amdgcn_raw_buffer_load_b64(rQ, (5 * s + 2 * h) * 16384 + tok, 0, 16);
        const u32x2 c = __builtin_amdgcn_raw_buffer_load_b64(rQ, (5 * s + 2 * h + 1) * 16384 + tok, 0, 16);
        const u32x2 d = __builtin_amdgcn_raw_buffer_load_b64(rQ, h ? OOB : (5 * s + 4) * 16384 + tok, 0, 16);
        q1 = cat(__builtin_bit_cast(s16x4, a), __builtin_bit_cast(s16x4, c));
        q2 = __builtin_bit_cast(s16x4, d);
      }
      BAR();
      STAMP(16);
      const u32 kadr = L0 + LK + 2 * h * PL + (32 * w + q) * 8;
      const u32 k2adr = L0 + LK + 4 * PL + (32 * w + q) * 8;
      const u32 vadr = L0 + LV + (4 * (g & 1) + p4) * PL + (32 * w + 4 * h + q4) * 8;
      const f32x16 zero16 = {0, 0, 0, 0, 0, 0, 0, 0, 0, 0, 0, 0, 0, 0, 0, 0};
      const int qd = q - 4 * h;
      constexpr float NEG = -3.0e38f;
      auto score = [&](int j, const f32x16& c) -> f32x16 {
        return mfma32k8(lds_b64(k2adr + j * 256), q2, mfma32(cat(lds_b64(kadr + j * 256), lds_b64(kadr + PL + j * 256)), q1, c));
      };
      auto pv = [&](int j, int ks, u32 p0, u32 p1, u32 p2, u32 p3, const f32x16& o) -> f32x16 {
        const u32 a = vadr + j * 256 + ks * 128;
        return mfma32(cat(lds_tr(a), lds_tr(a + 64)), __builtin_bit_cast(s16x8, (u32x4){p0, p1, p2, p3}), o);
      };
      f32x16 o = zero16;
      float m = NEG;
      const int jstart = T > 0 ? 1 : (8 - w > 1 ? 8 - w : 1);
      if (T > 0) {
        const f32x16 sf = score(0, zero16), sd = score(8, zero16);
#pragma unroll
        for (int r = 0; r < 16; r += 2) {
          const float e0 = ((r & 3) + 8 * (r >> 2) <= qd) ? sd[r] : sf[r], e1 = (((r + 1) & 3) + 8 * ((r + 1) >> 2) <= qd) ? sd[r + 1] : sf[r + 1];
          m = fmaxf(fmaxf(m, e0), e1);
        }
#pragma unroll
        for (int j = 1; j < 8; ++j) {
          const f32x16 sc = score(j, zero16);
#pragma unroll
          for (int r = 0; r < 16; r += 2) m = fmaxf(fmaxf(m, sc[r]), sc[r + 1]);
        }
      } else {
        const f32x16 sd = score(8, zero16);
#pragma unroll
        for (int r = 0; r < 16; ++r) m = fmaxf(m, ((r & 3) + 8 * (r >> 2) <= qd) ? sd[r] : NEG);
#pragma unroll 1
        for (int j = jstart; j < 8; ++j) {
          const f32x16 sc = score(j, zero16);
#pragma unroll
          for (int r = 0; r < 16; r += 2) m = fmaxf(fmaxf(m, sc[r]), sc[r + 1]);
        }
      }
      m = xhalf_max(m);
      STAMP(17);
      asm volatile("" ::: "memory");
      f32x16 negm;
#pragma unroll
      for (int r = 0; r < 16; ++r) negm[r] = -m;
      auto full = [&](int j) {
        const f32x16 sc = score(j, negm);
        u32 pk[8];
#pragma unroll
        for (int i = 0; i < 8; ++i) pk[i] = pk_bf16(__builtin_amdgcn_exp2f(sc[2 * i]), __builtin_amdgcn_exp2f(sc[2 * i + 1]));
        o = pv(j, 0, pk[0], pk[1], pk[2], pk[3], o);
        o = pv(j, 1, pk[4], pk[5], pk[6], pk[7], o);
      };
      if (T > 0) {
#pragma unroll
        for (int j = 1; j < 8; ++j) full(j);
      } else {
#pragma unroll 1
        for (int j = jstart; j < 8; ++j) full(j);
      }
      {
        const f32x16 sd = score(8, negm);
        f32x16 sf;
        if (T > 0) sf = score(0, negm);
        else {
#pragma unroll
          for (int r = 0; r < 16; ++r) sf[r] = NEG;
        }
        u32 pd[8], pf[8];
#pragma unroll
        for (int i = 0; i < 8; ++i) {
          const bool v0 = ((2 * i) & 3) + 8 * ((2 * i) >> 2) <= qd, v1 = ((2 * i + 1) & 3) + 8 * ((2 * i + 1) >> 2) <= qd;
          const u32 pk = pk_bf16(__builtin_amdgcn_exp2f(v0 ? sd[2 * i] : sf[2 * i]), __builtin_amdgcn_exp2f(v1 ? sd[2 * i + 1] : sf[2 * i + 1]));
          const u32 mk = (v0 ? 0xffffu : 0u) | (v1 ? 0xffff0000u : 0u);
          pd[i] = pk & mk; pf[i] = pk & ~mk;
        }
        o = pv(8, 0, pd[0], pd[1], pd[2], pd[3], o);
        o = pv(8, 1, pd[4], pd[5], pd[6], pd[7], o);
        if (T > 0) {
          o = pv(0, 0, pf[0], pf[1], pf[2], pf[3], o);
          o = pv(0, 1, pf[4], pf[5], pf[6], pf[7], o);
        }
      }
      STAMP(18);
      auto r = __builtin_amdgcn_permlane32_swap(__float_as_uint(o[8]), __float_as_uint(o[8]), false, false);
      const float inv = 1.0f / __uint_as_float(r[1]);
      const __amdgpu_buffer_rsrc_t rO = mk_rs(ws + WS_O, SZ_O);
      const u32 tok = (u32)(256 * T + 32 * w + q) * 8;
#pragma unroll
      for (int i = 0; i < 3; ++i) {
        const u32x2 v = {pk_bf16(o[4 * i] * inv, o[4 * i + 1] * inv), pk_bf16(o[4 * i + 2] * inv, o[4 * i + 3] * inv)};
        __builtin_amdgcn_raw_buffer_store_b64(v, rO, (i == 2 && h) ? OOB : (u32)((5 * s + 2 * i + h) * 16384) + tok, 0, 16);
      }
      WAITV(0);
    }
    BAR();
    if (multi && tid == 0) sy.post(FL_ATT + T * 32 + s);
  }
  STAMP(3);

  u32 hres[3][2][2];
  const int wr3 = (w >> 1) & 1, wc3 = w & 1, kh = (w >> 2) & 1;
  u32 nb_adr[2];
  {
    const int sw = ((q4 >> 1) & 1) | ((g & 1) << 1);
#pragma unroll
    for (int j = 0; j < 2; ++j) nb_adr[j] = (8 * g + q4) * 128 + (((2 * wc3 + j) ^ sw) * 32) + 8 * p4;
  }
#define N_MMA(X, W) do { _Pragma("unroll") for (int ks = 0; ks < 2; ++ks) _Pragma("unroll") for (int i = 0; i < 3; ++i) _Pragma("unroll") for (int j = 0; j < 2; ++j) \
      acc[i][j] = mfma16(W[ks][j], X[ks][i], acc[i][j]); } while (0)
#define N_REDUCE(base) do { \
    if (kh) { _Pragma("unroll") for (int i = 0; i < 3; ++i) _Pragma("unroll") for (int j = 0; j < 2; ++j) *(LAS f32x4*)(size_t)((base) + ((w & 3) * 6 + i * 2 + j) * 1024 + lane * 16) = acc[i][j]; } \
    BAR(); \
    if (!kh) { _Pragma("unroll") for (int i = 0; i < 3; ++i) _Pragma("unroll") for (int j = 0; j < 2; ++j) acc[i][j] += *(LAS f32x4*)(size_t)((base) + ((w & 3) * 6 + i * 2 + j) * 1024 + lane * 16); } } while (0)

  if (ph_lo <= 3 && 3 < ph_hi) {
    if (own35) {
      constexpr u32 LA = 81920;
      u32x2 xr[3][2];
      if (w < 4) {
        const __amdgpu_buffer_rsrc_t rX = mk_rs(x, SEQ * DM * 2);
#pragma unroll
        for (int i = 0; i < 3; ++i)
#pragma unroll
          for (int j = 0; j < 2; ++j) {
            const int r = 48 * wr3 + 16 * i + l15;
            xr[i][j] = __builtin_amdgcn_raw_buffer_load_b64(rX, r < nrow3 ? (u32)((row3 + r) * 1280 + (64 * ct3 + 32 * wc3 + 16 * j + 4 * g) * 2) : OOB, 0, 0);
          }
      }
      if (!multi && w >= 8) prefetch_wo();
      if (multi && w == 0) sy.wait(lane < 32 ? FL_ATT + T * 32 + lane : -1);
      BAR();
      STAMP(4);
      if (w >= 8) {
        const u32x4 rO = mk_rsrc(ws + WS_O, SZ_O);
        u32 vA[3];
#pragma unroll
        for (int ii = 0; ii < 3; ++ii) {
          const int u = (lw * 3 + ii) * 64 + lane, pl = u / 48, un = (u - pl * 48) ^ (((pl >> 1) & 1) << 3);
          vA[ii] = 2 * un < nrow3 ? (u32)(pl * 16384 + (row3 + 2 * un) * 8) : OOB;
        }
        auto issue = [&](int c, int slot) {
#pragma unroll
          for (int ii = 0; ii < 3; ++ii) dma_c(rO, vA[ii], c * (16 * 16384), L0 + LA + slot * 12288 + (lw * 3 + ii) * 1024);
        };
#pragma unroll 1
        for (int c = 0; c < 6; ++c) issue(c, c);
        WAITV(12); BAR();
        WAITV(6);
#pragma unroll 1
        for (int i = 0; i < 5; ++i) {
          BAR();
          if (i < 2) { issue(2 * i + 6, 2 * i); issue(2 * i + 7, 2 * i + 1); WAITV(6); } else WAITV(0);
        }
        BAR();
      } else {
        f32x4 acc[3][2];
#pragma unroll
        for (int i = 0; i < 3; ++i) { acc[i][0] = (f32x4){0, 0, 0, 0}; acc[i][1] = (f32x4){0, 0, 0, 0}; }
        u32 aadr[3];
#pragma unroll
        for (int i = 0; i < 3; ++i) {
          const int tok = 48 * wr3 + 16 * i + l15;
          aadr[i] = LA + kh * 12288 + (2 * g) * 768 + (((tok >> 1) ^ ((g & 1) << 3)) * 16) + (tok & 1) * 8;
        }
        s16x8 xA[2][3], wA[2][2], xB[2][3], wB[2][2];
#define P3_READ(X, W, sa, sb) do { _Pragma("unroll") for (int ks = 0; ks < 2; ++ks) { \
          _Pragma("unroll") for (int j = 0; j < 2; ++j) { const u32 a_ = (sb) + nb_adr[j] + ks * 4096; W[ks][j] = cat(lds_tr(a_), lds_tr(a_ + 512)); } \
          _Pragma("unroll") for (int i = 0; i < 3; ++i) { const u32 a_ = (sa) + aadr[i] + ks * 8 * 768; X[ks][i] = cat(lds_b64(a_), lds_b64(a_ + 768)); } } } while (0)
#define P3_SCHED() do { _Pragma("unroll") for (int z = 0; z < 10; ++z) { SGB(M_DSR, 2); SGB(M_MFMA, 1); } SGB(M_MFMA, 2); SCHED0(); } while (0)
        const u32 wb = L0 + kh * 8192;
        BAR();
        P3_READ(xA, wA, L0, wb);
#pragma unroll 1
        for (int st = 0; st < 4; st += 2) {
          BAR(); SCHED0();
          P3_READ(xB, wB, L0 + (st == 0 ? 1 : 0) * 24576, wb + (st + 1) * 16384);
          N_MMA(xA, wA);
          P3_SCHED();
          BAR(); SCHED0();
          P3_READ(xA, wA, L0 + (st == 0 ? 2 : 1) * 24576, wb + (st + 2) * 16384);
          N_MMA(xB, wB);
          P3_SCHED();
        }
        BAR();
        N_MMA(xA, wA);
        N_REDUCE(L0);
        STAMP(19);
        if (w < 4) {
          const __amdgpu_buffer_rsrc_t rH = mk_rs(ws + WS_H, SZ_H);
#pragma unroll
          for (int i = 0; i < 3; ++i) {
            float hv[8];
#pragma unroll
            for (int j = 0; j < 2; ++j) {
              const u32 a0 = pk_bf16(acc[i][j][0], acc[i][j][1]), a1 = pk_bf16(acc[i][j][2], acc[i][j][3]);
              hres[i][j][0] = pk_bf16(bf_lo(xr[i][j][0]) + bf_lo(a0), bf_hi(xr[i][j][0]) + bf_hi(a0));
              hres[i][j][1] = pk_bf16(bf_lo(xr[i][j][1]) + bf_lo(a1), bf_hi(xr[i][j][1]) + bf_hi(a1));
              hv[4 * j] = bf_lo(hres[i][j][0]); hv[4 * j + 1] = bf_hi(hres[i][j][0]); hv[4 * j + 2] = bf_lo(hres[i][j][1]); hv[4 * j + 3] = bf_hi(hres[i][j][1]);
            }
            auto s0 = __builtin_amdgcn_permlane16_swap(hres[i][0][0], hres[i][1][0], false, false);
            auto s1 = __builtin_amdgcn_permlane16_swap(hres[i][0][1], hres[i][1][1], false, false);
            const int r = 48 * wr3 + 16 * i + l15;
            const int oct = 8 * ct3 + 4 * wc3 + ((g & 1) << 1) + (g >> 1);
            __builtin_amdgcn_raw_buffer_store_b128((u32x4){s0[0], s1[0], s0[1], s1[1]}, rH, r < nrow3 ? (u32)(oct * 32768 + (row3 + r) * 16) : OOB, 0, 16);
            float sm = 0.f;
#pragma unroll
            for (int e = 0; e < 8; ++e) sm += hv[e];
            const float mw = sum_x16_x32(sm) * (1.0f / 32.0f);
            float m2 = 0.f;
#pragma unroll
            for (int e = 0; e < 8; ++e) { const float d = hv[e] - mw; m2 += d * d; }
            m2 = sum_x16_x32(m2);
            if (g == 0) *(LAS f32x2*)(size_t)(L0 + L_TAB + (r * 2 + wc3) * 8) = (f32x2){mw, m2};
          }
        }
      }
      BAR();
      if (tid < nrow3) {
        const f32x4 p = *(LAS f32x4*)(size_t)(L0 + L_TAB + tid * 16);
        const float mt = 0.5f * (p[0] + p[2]), d0 = p[0] - mt, d1 = p[2] - mt;
        const float m2 = p[1] + p[3] + 32.0f * (d0 * d0 + d1 * d1);
        __hip_atomic_store((GAS u64*)(ws + WS_HST) + (T * 10 + ct3) * 256 + 96 * rt3 + tid, ((u64)__float_as_uint(m2) << 32) | __float_as_uint(mt), __ATOMIC_RELAXED, __HIP_MEMORY_SCOPE_AGENT);
      }
      if (w < 4) WAITV(0);
      BAR();
      if (multi && tid == 0) sy.post(FL_H + T * 30 + s);
    } else {
      colsum_job(wg, wu, ws, 4 * (2 * T + s - 30) + 2, L0 + 96 * 1024, tid, lane, w);
      if (multi && tid == 0) sy.post(FL_CS + 2 * T + s - 30);
    }
  }
  STAMP(5);

  if (ph_lo <= 4 && 4 < ph_hi) {
    if (s < 27) {
      constexpr u32 ST = 24576;
      if (w >= 8) {
        const u32x4 rA = mk_rsrc(ws + WS_H, SZ_H), rG = mk_rsrc(wg, DM * FF * 2), rU = mk_rsrc(wu, DM * FF * 2);
        const u32 vN = bn_voff(lw, FF * 2, s * 128);
        auto issueB = [&](int t, int slot) {
          dma(rG, vN, t * (32 * FF * 2), L0 + slot * ST + 16384 + lw * 1024);
          dma(rU, vN, t * (32 * FF * 2), L0 + slot * ST + 20480 + lw * 1024);
        };
        const u32 vA = (u32)((256 * T + lane) * 16);
        auto issueA = [&](int t, int slot) {
#pragma unroll
          for (int ii = 0; ii < 4; ++ii) dma_c(rA, vA, (4 * t + lw) * 32768 + ii * 1024, L0 + slot * ST + lw * 4096 + ii * 1024);
        };
#pragma unroll 1
        for (int t = 0; t < 6; ++t) issueB(t, t);
        BAR();
#pragma unroll 1
        for (int t = 0; t < 6; ++t) issueA(t, t);
        WAITV(20); BAR();
        WAITV(16);
        int slot = 0;
#pragma unroll 1
        for (int t = 0; t < 14; ++t) {
          BAR();
          issueA(t + 6, slot); issueB(t + 6, slot); slot = slot == 5 ? 0 : slot + 1;
          if (t < 3) WAITV(18); else WAITV(24);
        }
        asm volatile("" ::: "memory");
        const int row = tid - 512;
        const __amdgpu_buffer_rsrc_t rS = mk_rs(ws + WS_HST, SZ_HST), rC = mk_rs(ws + WS_CS, SZ_CS);
        u32x2 pp[10];
#pragma unroll
        for (int i = 0; i < 10; ++i) pp[i] = __builtin_amdgcn_raw_buffer_load_b64(rS, (u32)(((T * 10 + i) * 256 + row) * 8), 0, 16);
        const u32 csv = __builtin_amdgcn_raw_buffer_load_b32(rC, (u32)((((row >> 6) & 1) * 27 + s) * 256 + (row & 63) * 4), 0, 16);
        BAR(); WAITV(29);
        BAR(); WAITV(23);
        BAR(); WAITV(17);
        BAR(); WAITV(0);
        {
          float sm = 0.f;
#pragma unroll
          for (int i = 0; i < 10; ++i) sm += __uint_as_float(pp[i][0]);
          const float mean = sm * 0.1f;
          float m2 = 0.f;
#pragma unroll
          for (int i = 0; i < 10; ++i) { const float d = __uint_as_float(pp[i][0]) - mean; m2 += __uint_as_float(pp[i][1]) + 64.0f * d * d; }
          float m, r; ln_stats(mean, m2 * (1.0f / DM), m, r);
          *(LAS f32x2*)(size_t)(L0 + L_TAB + row * 8) = (f32x2){m, r};
          if (row < 128) *(LAS u32*)(size_t)(L0 + L_TAB + 2048 + row * 4) = csv;
        }
        BAR(); BAR();
        BAR();
      } else {
        if (multi && w == 0) {
          int idx = -1;
          if (lane < 30) idx = FL_H + T * 30 + lane;
          else if (lane == 30) idx = FL_CS + (s >> 2);
          else if (lane == 31) idx = FL_CS + ((27 + s) >> 2);
          sy.wait(idx);
        }
        BAR();
        STAMP(6);
        const int wr = w >> 1, wc = w & 1;
        f32x4 ag[4][2], au[4][2];
#pragma unroll
        for (int i = 0; i < 4; ++i)
#pragma unroll
          for (int j = 0; j < 2; ++j) { ag[i][j] = (f32x4){0, 0, 0, 0}; au[i][j] = (f32x4){0, 0, 0, 0}; }
        const u32 xadr = g * 4096 + (64 * wr + l15) * 16;
        u32 wadr[2];
        {
          const int sw = ((q4 >> 1) & 1) | ((g & 1) << 1);
#pragma unroll
          for (int j = 0; j < 2; ++j) wadr[j] = 16384 + (8 * g + q4) * 128 + (((2 * wc + j) ^ sw) * 32) + 8 * p4;
        }
        s16x8 xA[4], gA[2], uA[2], xB[4], gB[2], uB[2];
#define P4_READ(X, G_, U_, base) do { _Pragma("unroll") for (int i = 0; i < 4; ++i) X[i] = lds_b128((base) + xadr + i * 256); \
          _Pragma("unroll") for (int j = 0; j < 2; ++j) { const u32 a_ = (base) + wadr[j]; G_[j] = cat(lds_tr(a_), lds_tr(a_ + 512)); U_[j] = cat(lds_tr(a_ + 4096), lds_tr(a_ + 4096 + 512)); } } while (0)
#define P4_MMA(X, G_, U_) do { _Pragma("unroll") for (int i = 0; i < 4; ++i) _Pragma("unroll") for (int j = 0; j < 2; ++j) { \
          ag[i][j] = mfma16(G_[j], X[i], ag[i][j]); au[i][j] = mfma16(U_[j], X[i], au[i][j]); } } while (0)
#define P4_SCHED() do { _Pragma("unroll") for (int z = 0; z < 6; ++z) { SGB(M_DSR, 2); SGB(M_MFMA, 2); } SGB(M_MFMA, 4); SCHED0(); } while (0)
        BAR();
        STAMP(20);
        CSTAMP(26);
        P4_READ(xA, gA, uA, L0);
        u32 base = L0 + ST;
#pragma unroll 1
        for (int t = 0; t < 20; t += 2) {
          BAR(); SCHED0();
          P4_READ(xB, gB, uB, base);
          P4_MMA(xA, gA, uA);
          P4_SCHED();
          base = base == L0 + 5 * ST ? L0 : base + ST;
          BAR(); SCHED0();
          P4_READ(xA, gA, uA, base);
          P4_MMA(xB, gB, uB);
          P4_SCHED();
          base = base == L0 + 5 * ST ? L0 : base + ST;
        }
        BAR();
        STAMP(21);
        CSTAMP(27);
        const __amdgpu_buffer_rsrc_t rH = mk_rs(ws + WS_G, SZ_G);
        f32x4 cg[2], cu[2];
#pragma unroll
        for (int j = 0; j < 2; ++j) {
          cg[j] = *(LAS f32x4*)(size_t)(L0 + L_TAB + 2048 + (32 * wc + 16 * j + 4 * g) * 4);
          cu[j] = *(LAS f32x4*)(size_t)(L0 + L_TAB + 2048 + 256 + (32 * wc + 16 * j + 4 * g) * 4);
        }
#pragma unroll
        for (int i = 0; i < 4; ++i) {
          const int row = 64 * wr + 16 * i + l15;
          const f32x2 st = *(LAS f32x2*)(size_t)(L0 + L_TAB + row * 8);
          const float r2 = st[1] * st[1];
          u32 pk[2][2];
#pragma unroll
          for (int j = 0; j < 2; ++j) {
            f32x4 a = ag[i][j] - cg[j] * st[0], u = (au[i][j] - cu[j] * st[0]) * r2;
#pragma unroll
            for (int e = 0; e < 4; ++e) a[e] = fmaxf(a[e], 0.f) * u[e];
            pk[j][0] = pk_bf16(a[0], a[1]); pk[j][1] = pk_bf16(a[2], a[3]);
          }
          auto s0 = __builtin_amdgcn_permlane16_swap(pk[0][0], pk[1][0], false, false);
          auto s1 = __builtin_amdgcn_permlane16_swap(pk[0][1], pk[1][1], false, false);
          const int oct = 8 * s + 4 * wc + ((g & 1) << 1) + (g >> 1);
          __builtin_amdgcn_raw_buffer_store_b128((u32x4){s0[0], s1[0], s0[1], s1[1]}, rH, (u32)(oct * 32768 + (256 * T + row) * 16), 0, 16);
        }
        STAMP(22);
        WAITV(0);
      }
      BAR();
      if (multi && tid == 0) sy.post(FL_FF + T * 27 + s);
    }
  }
  STAMP(7);

  if (ph_lo <= 5 && 5 < ph_hi) {
    if (own35) {
      constexpr u32 LA = 81920;
      if (w >= 8) {
        const u32x4 rW = mk_rsrc(wd, FF * DM * 2), rG = mk_rsrc(ws + WS_G, SZ_G);
        const u32 vB0 = bn_voff(2 * lw, 1280, ct3 * 128), vB1 = bn_voff(2 * lw + 1, 1280, ct3 * 128);
        auto issueB = [&](int i, int slot) {
#pragma unroll
          for (int cc = 0; cc < 2; ++cc) {
            const int c = 2 * i + cc > 26 ? 26 : 2 * i + cc;
            dma(rW, vB0, c * (64 * 1280), L0 + slot * 16384 + cc * 8192 + lw * 2048);
            dma(rW, vB1, c * (64 * 1280), L0 + slot * 16384 + cc * 8192 + lw * 2048 + 1024);
          }
        };
#pragma unroll 1
        for (int i = 0; i < 5; ++i) issueB(i, i);
        u32 vA[3];
#pragma unroll
        for (int ii = 0; ii < 3; ++ii) {
          const int u = (lw * 3 + ii) * 64 + lane, pl = u / 96, tk = u - pl * 96;
          vA[ii] = tk < nrow3 ? (u32)(pl * 32768 + (row3 + tk) * 16) : OOB;
        }
        auto issueA = [&](int i, int slot) {
#pragma unroll
          for (int cc = 0; cc < 2; ++cc) {
            const int c = 2 * i + cc > 26 ? 26 : 2 * i + cc;
#pragma unroll
            for (int ii = 0; ii < 3; ++ii) dma_c(rG, vA[ii], c * (8 * 32768), L0 + LA + slot * 24576 + cc * 12288 + (lw * 3 + ii) * 1024);
          }
        };
        BAR();
#pragma unroll 1
        for (int i = 0; i < 3; ++i) issueA(i, i);
        WAITV(12); BAR();
        WAITV(6);
        int sa = 0, sb = 0;
#pragma unroll 1
        for (int i = 0; i < 14; ++i) {
          BAR();
          if (i <= 10) { issueA(i + 3, sa); sa = sa == 2 ? 0 : sa + 1; }
          if (i <= 8) { issueB(i + 5, sb); sb = sb == 4 ? 0 : sb + 1; }
          if (i >= 1 && i <= 8) WAITV(14); else if (i == 0 || i == 9) WAITV(10); else if (i == 10) WAITV(6); else WAITV(0);
        }
        BAR();
      } else {
        if (!multi && w < 4) {
          const __amdgpu_buffer_rsrc_t rH = mk_rs(ws + WS_H, SZ_H);
#pragma unroll
          for (int i = 0; i < 3; ++i)
#pragma unroll
            for (int j = 0; j < 2; ++j) {
              const int r = 48 * wr3 + 16 * i + l15, col = 64 * ct3 + 32 * wc3 + 16 * j + 4 * g;
              const u32x2 v = __builtin_amdgcn_raw_buffer_load_b64(rH, r < nrow3 ? (u32)((col >> 3) * 32768 + (row3 + r) * 16 + (col & 7) * 2) : OOB, 0, 0);
              hres[i][j][0] = v[0]; hres[i][j][1] = v[1];
            }
        }
        if (multi && w == 0) sy.wait(lane < 27 ? FL_FF + T * 27 + lane : -1);
        BAR();
        STAMP(8);
        f32x4 acc[3][2];
#pragma unroll
        for (int i = 0; i < 3; ++i) { acc[i][0] = (f32x4){0, 0, 0, 0}; acc[i][1] = (f32x4){0, 0, 0, 0}; }
        const u32 aadr = LA + kh * 12288 + g * 1536 + (48 * wr3 + l15) * 16;
        s16x8 xA[2][3], wA[2][2], xB[2][3], wB[2][2];
#define P5_READ(X, W, sa, sb) do { _Pragma("unroll") for (int ks = 0; ks < 2; ++ks) { \
          _Pragma("unroll") for (int j = 0; j < 2; ++j) { const u32 a_ = (sb) + nb_adr[j] + ks * 4096; W[ks][j] = cat(lds_tr(a_), lds_tr(a_ + 512)); } \
          _Pragma("unroll") for (int i = 0; i < 3; ++i) X[ks][i] = lds_b128((sa) + aadr + ks * 4 * 1536 + i * 256); } } while (0)
#define P5_SCHED() do { _Pragma("unroll") for (int z = 0; z < 7; ++z) { SGB(M_DSR, 2); SGB(M_MFMA, 1); } SGB(M_MFMA, 5); SCHED0(); } while (0)
        BAR();
        P5_READ(xA, wA, L0, L0 + kh * 8192);
        u32 pa = L0 + 24576, pb = L0 + kh * 8192 + 16384;
        const u32 pa_end = L0 + 2 * 24576, pb_end = L0 + kh * 8192 + 4 * 16384;
#pragma unroll 1
        for (int i = 0; i < 12; i += 2) {
          BAR(); SCHED0();
          P5_READ(xB, wB, pa, pb);
          N_MMA(xA, wA);
          P5_SCHED();
          pa = pa == pa_end ? L0 : pa + 24576; pb = pb == pb_end ? L0 + kh * 8192 : pb + 16384;
          BAR(); SCHED0();
          P5_READ(xA, wA, pa, pb);
          N_MMA(xB, wB);
          P5_SCHED();
          pa = pa == pa_end ? L0 : pa + 24576; pb = pb == pb_end ? L0 + kh * 8192 : pb + 16384;
        }
        BAR(); SCHED0();
        P5_READ(xB, wB, pa, pb);
        N_MMA(xA, wA);
        P5_SCHED();
        BAR();
        if (!kh) N_MMA(xB, wB);
        N_REDUCE(L0);
        STAMP(23);
        if (w < 4) {
#pragma unroll
          for (int i = 0; i < 3; ++i) {
            u32 pk[2][2];
#pragma unroll
            for (int j = 0; j < 2; ++j) {
              const u32 a0 = pk_bf16(acc[i][j][0], acc[i][j][1]), a1 = pk_bf16(acc[i][j][2], acc[i][j][3]);
              pk[j][0] = pk_bf16(bf_lo(hres[i][j][0]) + bf_lo(a0), bf_hi(hres[i][j][0]) + bf_hi(a0));
              pk[j][1] = pk_bf16(bf_lo(hres[i][j][1]) + bf_lo(a1), bf_hi(hres[i][j][1]) + bf_hi(a1));
            }
            auto s0 = __builtin_amdgcn_permlane16_swap(pk[0][0], pk[1][0], false, false);
            auto s1 = __builtin_amdgcn_permlane16_swap(pk[0][1], pk[1][1], false, false);
            const int r = 48 * wr3 + 16 * i + l15;
            const int oct = 8 * ct3 + 4 * wc3 + ((g & 1) << 1) + (g >> 1);
            if (r < nrow3) *(u32x4*)(out + (size_t)(row3 + r) * DM + oct * 8) = (u32x4){s0[0], s1[0], s0[1], s1[1]};
          }
        }
      }
    }
    if (multi && b == 0 && w == 0) {
#pragma unroll 1
      for (int i = 0; i < 4; ++i) sy.wait(FL_ATT + i * 64 + lane);
      if (lane == 0) __hip_atomic_store((GAS u64*)(ws + WS_EPOCH), sy.tag, __ATOMIC_RELAXED, __HIP_MEMORY_SCOPE_AGENT);
    }
  }
  STAMP(9);
}

extern "C" void kernel_launch(void* const* d_in, const int* in_sizes, int n_in, void* d_out, int out_size, void* d_ws, size_t ws_size, hipStream_t stream) {
  (void)in_sizes; (void)n_in; (void)out_size; (void)ws_size;
  const u16* x = (const u16*)d_in[0]; const u16* wqkv = (const u16*)d_in[1]; const u16* wo = (const u16*)d_in[2];
  const u16* wg = (const u16*)d_in[4]; const u16* wu = (const u16*)d_in[5]; const u16* wd = (const u16*)d_in[6];
  (void)hipFuncSetAttribute((const void*)fused_block, hipFuncAttributeMaxDynamicSharedMemorySize, LDS_BYTES);
  fused_block<<<256, NTHR, LDS_BYTES, stream>>>(x, wqkv, wo, wg, wu, wd, (u16*)d_out, (char*)d_ws, 1, 6);
}
```

```cpp
#include <hip/hip_runtime.h>
#include <stdint.h>

typedef unsigned short u16;
typedef unsigned int u32;
typedef unsigned long long u64;
typedef short s16x4 __attribute__((ext_vector_type(4)));
typedef short s16x8 __attribute__((ext_vector_type(8)));
typedef __bf16 bf16x8 __attribute__((ext_vector_type(8)));
typedef __bf16 bf16x2 __attribute__((ext_vector_type(2)));
typedef float f32x2 __attribute__((ext_vector_type(2)));
typedef float f32x4 __attribute__((ext_vector_type(4)));
typedef float f32x16 __attribute__((ext_vector_type(16)));
typedef u32 u32x2 __attribute__((ext_vector_type(2)));
typedef u32 u32x4 __attribute__((ext_vector_type(4)));
#define LAS __attribute__((address_space(3)))
#define GAS __attribute__((address_space(1)))
#define DEV __device__ __forceinline__

#define NLAUNCH 1
#define STAMPS 0

constexpr int SEQ = 2048, DM = 640, FF = 1728;
constexpr u32 WS_EPOCH = 0;
constexpr u32 WS_FLAGS = 4096;
constexpr u32 FL_QKV = 0, FL_ATT = 256, FL_H = 512, FL_FF = 768, FL_CS = 1000;
constexpr u32 WS_QKV = 256 * 1024;
constexpr u32 SZ_QKV = 480 * 16384;
constexpr u32 WS_O = WS_QKV + SZ_QKV;
constexpr u32 SZ_O = 160 * 16384;
constexpr u32 WS_H = WS_O + SZ_O;
constexpr u32 SZ_H = 80 * 32768;
constexpr u32 WS_HST = WS_H + SZ_H;
constexpr u32 SZ_HST = 8 * 10 * 256 * 8;
constexpr u32 WS_G = WS_HST + SZ_HST;
constexpr u32 SZ_G = 216 * 32768;
constexpr u32 WS_CS = WS_G + SZ_G;
constexpr u32 SZ_CS = 54 * 64 * 4;
constexpr u32 WS_STAMP = WS_CS + 16384;
constexpr u32 LDS_BYTES = 160 * 1024;
constexpr u32 L_TAB = 156 * 1024;
constexpr int NTHR = 768;
constexpr u32 OOB = 0x80000000u;
constexpr int CODE_KB = 30;

DEV u32x4 mk_rsrc(const void* p, u32 bytes) {
  const u64 a = (u64)p;
  u32x4 r; r[0] = (u32)a; r[1] = (u32)(a >> 32) & 0xffffu; r[2] = bytes; r[3] = 0x00020000u; return r;
}
DEV __amdgpu_buffer_rsrc_t mk_rs(const void* p, u32 bytes) { return __builtin_amdgcn_make_buffer_rsrc((void*)p, 0, bytes, 0x00020000); }
DEV void dma(u32x4 rs, u32 voff, u32 soff, u32 lds) {
  asm volatile("s_mov_b32 m0, %0\n\ts_nop 0\n\tbuffer_load_dwordx4 %1, %2, %3 offen lds" ::"s"(lds), "v"(voff), "s"(rs), "s"(soff));
}
DEV void dma_c(u32x4 rs, u32 voff, u32 soff, u32 lds) {
  asm volatile("s_mov_b32 m0, %0\n\ts_nop 0\n\tbuffer_load_dwordx4 %1, %2, %3 offen sc1 lds" ::"s"(lds), "v"(voff), "s"(rs), "s"(soff));
}
#define WAITV(n) asm volatile("s_waitcnt vmcnt(" #n ")" ::: "memory")
#define BAR() asm volatile("s_waitcnt lgkmcnt(0)\n\ts_barrier" ::: "memory")
DEV u32 pk_bf16(float lo, float hi) { f32x2 v = {lo, hi}; return __builtin_bit_cast(u32, __builtin_convertvector(v, bf16x2)); }
DEV float bf_lo(u32 d) { return __uint_as_float(d << 16); }
DEV float bf_hi(u32 d) { return __uint_as_float(d & 0xffff0000u); }
DEV float rbf(float x) { return bf_lo(pk_bf16(x, x)); }
DEV s16x8 lds_b128(u32 a) { return *(LAS s16x8*)(size_t)a; }
DEV s16x4 lds_b64(u32 a) { return *(LAS s16x4*)(size_t)a; }
DEV s16x4 lds_tr(u32 a) { return __builtin_amdgcn_ds_read_tr16_b64_v4i16((LAS s16x4*)(size_t)a); }
DEV s16x8 cat(s16x4 a, s16x4 b) { return __builtin_shufflevector(a, b, 0, 1, 2, 3, 4, 5, 6, 7); }
DEV f32x4 mfma16(s16x8 a, s16x8 b, f32x4 c) { return __builtin_amdgcn_mfma_f32_16x16x32_bf16(__builtin_bit_cast(bf16x8, a), __builtin_bit_cast(bf16x8, b), c, 0, 0, 0); }
DEV f32x16 mfma32(s16x8 a, s16x8 b, f32x16 c) { return __builtin_amdgcn_mfma_f32_32x32x16_bf16(__builtin_bit_cast(bf16x8, a), __builtin_bit_cast(bf16x8, b), c, 0, 0, 0); }
DEV f32x16 mfma32k8(s16x4 a, s16x4 b, f32x16 c) { return __builtin_amdgcn_mfma_f32_32x32x8bf16_1k(a, b, c, 0, 0, 0); }
DEV float dot2(u32 a, u32 b, float c) { return __builtin_amdgcn_fdot2_f32_bf16(__builtin_bit_cast(bf16x2, a), __builtin_bit_cast(bf16x2, b), c, false); }
DEV float xhalf_max(float m) { auto r = __builtin_amdgcn_permlane32_swap(__float_as_uint(m), __float_as_uint(m), false, false); return fmaxf(__uint_as_float(r[0]), __uint_as_float(r[1])); }
DEV float sum_x16_x32(float s) { s += __shfl_xor(s, 16); s += __shfl_xor(s, 32); return s; }
DEV void ln_stats(float mean, float var, float& m_out, float& r_out) {
  m_out = rbf(mean);
  const float sd = rbf(__builtin_amdgcn_sqrtf(rbf(rbf(var) + 1e-5f)));
  r_out = __builtin_amdgcn_rcpf(sd);
}

struct Sync {
  GAS u64* flags; u64 tag;
  DEV void wait(int idx) const {
    GAS u64* p = flags + (idx < 0 ? 0 : idx) * 16;
    for (u32 spins = 0; spins < (1u << 16); ++spins) {
      const u64 v = __hip_atomic_load(p, __ATOMIC_RELAXED, __HIP_MEMORY_SCOPE_AGENT);
      if (__all(idx < 0 || v == tag)) break;
      __builtin_amdgcn_s_sleep(1);
    }
  }
  DEV void post(int idx) const { __hip_atomic_store(flags + idx * 16, tag, __ATOMIC_RELAXED, __HIP_MEMORY_SCOPE_AGENT); }
};

#define CSTAMP(i) do {} while (0)
#define STAMP(i) do {} while (0)
#define STAMPW(i, wv) do {} while (0)

DEV void colsum_job(const u16* wg, const u16* wu, char* ws, int G0, u32 lds0, int tid, int lane, int w) {
  if (w < 8) {
  u32x4 v[2][10];
#pragma unroll
  for (int sg = 0; sg < 2; ++sg) {
    int G = G0 + sg; if (G > 53) G = 53;
    const u16* W = (G < 27 ? wg + G * 64 : wu + (G - 27) * 64) + (lane & 7) * 8 + (size_t)(8 * w + (lane >> 3)) * FF;
#pragma unroll
    for (int i = 0; i < 10; ++i) v[sg][i] = *(const u32x4*)(W + (size_t)i * 64 * FF);
  }
#pragma unroll
  for (int sg = 0; sg < 2; ++sg) {
    float a[8];
#pragma unroll
    for (int e = 0; e < 8; ++e) a[e] = 0.f;
#pragma unroll
    for (int i = 0; i < 10; ++i)
#pragma unroll
      for (int e = 0; e < 4; ++e) { a[2 * e] += bf_lo(v[sg][i][e]); a[2 * e + 1] += bf_hi(v[sg][i][e]); }
#pragma unroll
    for (int e = 0; e < 8; ++e) { float s = a[e]; s += __shfl_xor(s, 8); s = sum_x16_x32(s); a[e] = s; }
    if (lane < 8) {
      LAS f32x4* p = (LAS f32x4*)(size_t)(lds0 + ((sg * 8 + w) * 64 + lane * 8) * 4);
      p[0] = (f32x4){a[0], a[1], a[2], a[3]}; p[1] = (f32x4){a[4], a[5], a[6], a[7]};
    }
  }
  }
  BAR();
  if (tid < 128) {
    const int sg = tid >> 6, c = tid & 63; float s = 0.f;
#pragma unroll
    for (int ww = 0; ww < 8; ++ww) s += *(LAS float*)(size_t)(lds0 + ((sg * 8 + ww) * 64 + c) * 4);
    if (G0 + sg < 54) __hip_atomic_store((GAS float*)(ws + WS_CS) + (G0 + sg) * 64 + c, s, __ATOMIC_RELAXED, __HIP_MEMORY_SCOPE_AGENT);
  }
  WAITV(0);
  BAR();
}

#define SCHED0() __builtin_amdgcn_sched_barrier(0)
#define SGB(mask, n) __builtin_amdgcn_sched_group_barrier(mask, n, 0)
#define PBAR() asm volatile("s_barrier" ::: "memory")
constexpr int M_MFMA = 0x8, M_DSR = 0x100, M_VALU = 0x2;

__global__ __launch_bounds__(768, 3) void fused_block(const u16* __restrict__ x, const u16* __restrict__ wqkv, const u16* __restrict__ wo,
                                                      const u16* __restrict__ wg, const u16* __restrict__ wu, const u16* __restrict__ wd,
                                                      u16* __restrict__ out, char* __restrict__ ws, int ph_lo, int ph_hi) {
  extern __shared__ __attribute__((aligned(16))) char smem[];
  const u32 L0 = (u32)(size_t)(LAS char*)smem;
  const int tid = threadIdx.x, lane = tid & 63;
  const int w = __builtin_amdgcn_readfirstlane(tid >> 6);
  const int lw = w - 8;
  const int b = blockIdx.x, T = b & 7, s = b >> 3;
  const int l15 = lane & 15, g = lane >> 4, q4 = l15 >> 2, p4 = lane & 3;
  Sync sy; sy.flags = (GAS u64*)(ws + WS_FLAGS); sy.tag = 0;
  const bool multi = (NLAUNCH == 1);
  if (multi && w == 0) {
    const u64 e = __hip_atomic_load((GAS u64*)(ws + WS_EPOCH), __ATOMIC_RELAXED, __HIP_MEMORY_SCOPE_AGENT) + 1;
    sy.tag = ((u64)(u32)__builtin_amdgcn_readfirstlane((u32)(e >> 32)) << 32) | (u32)__builtin_amdgcn_readfirstlane((u32)e);
  }
  u32x4 pfd = {0, 0, 0, 0};
  if (s == 0 && w == 7) {
    u64 pc; asm volatile("s_getpc_b64 %0" : "=s"(pc));
    const char* p = (const char*)(pc & ~127ull) + lane * 16;
#pragma unroll 1
    for (int i = 0; i < CODE_KB; ++i) asm volatile("global_load_dwordx4 %0, %1, off" : "+v"(pfd) : "v"(p + i * 1024));
  }
  STAMP(0);
  const bool own35 = s < 30;
  const int rt3 = s / 10, ct3 = s - rt3 * 10;
  const int nrow3 = rt3 < 2 ? 96 : 64;
  const int row3 = 256 * T + 96 * rt3;

  auto bn_voff = [&](int i8, int pitch, int colb) -> u32 {
    const int k = 8 * i8 + (lane >> 3);
    const int sw = ((k >> 1) & 1) | (((k >> 3) & 1) << 1);
    return (u32)(k * pitch + colb + ((((lane & 7) >> 1) ^ sw) * 32) + (lane & 1) * 16);
  };
  auto prefetch_wo = [&]() {
    const u32x4 rW = mk_rsrc(wo, DM * DM * 2);
    const u32 v0 = bn_voff(0, 1280, ct3 * 128), v1 = bn_voff(1, 1280, ct3 * 128);
#pragma unroll 1
    for (int i = lw * 20; i < lw * 20 + 20; i += 2) {
      dma(rW, v0, i * 8 * 1280, L0 + i * 1024);
      dma(rW, v1, i * 8 * 1280, L0 + i * 1024 + 1024);
    }
  };

  if (ph_lo <= 1 && 1 < ph_hi) {
    if (!(T & 1) || s < 28) {
      const int RT = 4 * (T >> 1) + (s & 3), ct = 8 * (T & 1) + (s >> 2);
      constexpr u32 TAB = 120 * 1024;
      if (w >= 8) {
        const u32x4 rA = mk_rsrc(x, SEQ * DM * 2), rB = mk_rsrc(wqkv, DM * 3 * DM * 2);
        u32 vA[4], vB[4];
#pragma unroll
        for (int ii = 0; ii < 4; ++ii) {
          const int i = lw * 4 + ii;
          const int row = 8 * i + (lane >> 3), c = (lane & 7) ^ ((row >> 1) & 7);
          vA[ii] = (u32)((128 * RT + row) * 1280 + c * 16);
          const int k = 4 * i + g;
          vB[ii] = (u32)(k * 3840 + ct * 256 + (((l15 >> 2) ^ (k & 3)) * 64) + (l15 & 3) * 16);
        }
        auto issue = [&](int c, int slot) {
          const u32 base = L0 + slot * 32768 + lw * 4096;
#pragma unroll
          for (int ii = 0; ii < 4; ++ii) dma(rA, vA[ii], c * 128, base + ii * 1024);
#pragma unroll
          for (int ii = 0; ii < 4; ++ii) dma(rB, vB[ii], c * (64 * 3840), base + 16384 + ii * 1024);
        };
#pragma unroll 1
        for (int c = 0; c < 4; ++c) issue(c, c);
        STAMPW(10, 8);
        WAITV(24); BAR();
        STAMPW(11, 8);
        WAITV(16);
        int slot = 4;
#pragma unroll 1
        for (int c = 0; c < 10; ++c) {
          BAR();
          if (c < 6) { issue(c + 4, slot); slot = slot == 4 ? 0 : slot + 1; WAITV(16); }
          else if (c == 6) WAITV(8); else WAITV(0);
        }
        BAR();
      } else if (w >= 4) {
        const int hw = w - 4;
        f32x4 s1[2] = {{0, 0, 0, 0}, {0, 0, 0, 0}}, s2[2] = {{0, 0, 0, 0}, {0, 0, 0, 0}}, cs[2] = {{0, 0, 0, 0}, {0, 0, 0, 0}};
        const s16x8 ones = {0x3f80, 0x3f80, 0x3f80, 0x3f80, 0x3f80, 0x3f80, 0x3f80, 0x3f80};
        const u32 wadr = 16384 + (8 * g + q4) * 256 + ((hw ^ q4) * 64) + 8 * p4;
        const int f7 = (l15 >> 1) & 7;
        const u32 xadr0 = (32 * hw + l15) * 128 + ((g ^ f7) * 16), xadr1 = (32 * hw + l15) * 128 + (((4 + g) ^ f7) * 16);
        BAR();
        u32 base = L0;
#pragma unroll 1
        for (int c = 0; c < 10; ++c) {
          BAR();
#pragma unroll
          for (int ks = 0; ks < 2; ++ks)
#pragma unroll
            for (int j = 0; j < 2; ++j) {
              const s16x8 xf = lds_b128(base + (ks ? xadr1 : xadr0) + j * 2048);
              const u32 a = base + wadr + ks * 8192 + j * 32;
              s1[j] = mfma16(ones, xf, s1[j]); s2[j] = mfma16(xf, xf, s2[j]);
              cs[j] = mfma16(cat(lds_tr(a), lds_tr(a + 1024)), ones, cs[j]);
            }
          base = base == L0 + 4 * 32768 ? L0 : base + 32768;
        }
#pragma unroll
        for (int j = 0; j < 2; ++j) {
          const float sq = (l15 & 2) ? ((l15 & 1) ? s2[j][3] : s2[j][2]) : ((l15 & 1) ? s2[j][1] : s2[j][0]);
          const float mean = s1[j][0] * (1.0f / DM), var = fmaxf(sq * (1.0f / DM) - mean * mean, 0.f);
          float m, r; ln_stats(mean, var, m, r);
          if (g == q4) *(LAS f32x2*)(size_t)(L0 + TAB + (32 * hw + 16 * j + l15) * 8) = (f32x2){m, r};
          if (l15 == 0) *(LAS f32x4*)(size_t)(L0 + TAB + 1024 + ((2 * hw + j) * 16 + 4 * g) * 4) = cs[j];
        }
        BAR();
      } else {
        const int wr = w >> 1, wc = w & 1, r31 = lane & 31, h = lane >> 5, blk = g & 1;
        f32x16 acc[2][2];
#pragma unroll
        for (int i = 0; i < 2; ++i)
#pragma unroll
          for (int j = 0; j < 2; ++j)
#pragma unroll
            for (int r = 0; r < 16; ++r) acc[i][j][r] = 0.f;
        u32 xa[4], wa[2];
        {
          const int row = 64 * wr + r31, f = (row >> 1) & 7;
#pragma unroll
          for (int kk = 0; kk < 4; ++kk) xa[kk] = row * 128 + (((2 * kk + h) ^ f) * 16);
#pragma unroll
          for (int j = 0; j < 2; ++j) wa[j] = 16384 + (8 * h + q4) * 256 + (((2 * wc + j) ^ q4) * 64) + blk * 32 + 8 * p4;
        }
        s16x8 xA[2][2], wA[2][2], xB[2][2], wB[2][2];
#define P1_READ(X, W, base, ks) do { _Pragma("unroll") for (int kk = 0; kk < 2; ++kk) { \
          _Pragma("unroll") for (int j = 0; j < 2; ++j) { const u32 a_ = (base) + wa[j] + (2 * (ks) + kk) * 4096; W[kk][j] = cat(lds_tr(a_), lds_tr(a_ + 1024)); } \
          _Pragma("unroll") for (int i = 0; i < 2; ++i) X[kk][i] = lds_b128((base) + xa[2 * (ks) + kk] + i * 4096); } } while (0)
#define P1_MMA(X, W) do { _Pragma("unroll") for (int kk = 0; kk < 2; ++kk) _Pragma("unroll") for (int i = 0; i < 2; ++i) _Pragma("unroll") for (int j = 0; j < 2; ++j) \
          acc[i][j] = mfma32(W[kk][j], X[kk][i], acc[i][j]); } while (0)
#define P1_SCHED() do { _Pragma("unroll") for (int z = 0; z < 6; ++z) { SGB(M_MFMA, 1); SGB(M_DSR, 2); } SGB(M_MFMA, 2); SCHED0(); } while (0)
        BAR();
        CSTAMP(28);
        P1_READ(xA, wA, L0, 0);
        u32 base = L0;
#pragma unroll 1
        for (int c = 0; c < 10; ++c) {
          PBAR(); SCHED0();
          P1_READ(xB, wB, base, 1);
          P1_MMA(xA, wA);
          P1_SCHED();
          base = base == L0 + 4 * 32768 ? L0 : base + 32768;
          P1_READ(xA, wA, base, 0);
          P1_MMA(xB, wB);
          P1_SCHED();
        }
        STAMP(12);
        CSTAMP(29);
        BAR();
        STAMP(13);
        const float qs = ct < 5 ? 0.22360679775f * 1.44269504089f : 1.0f;
        const __amdgpu_buffer_rsrc_t rO = mk_rs(ws + WS_QKV, SZ_QKV);
#pragma unroll
        for (int i = 0; i < 2; ++i) {
          const int row = 64 * wr + 32 * i + r31;
          const f32x2 st = *(LAS f32x2*)(size_t)(L0 + TAB + row * 8);
          const float rr = st[1] * qs;
#pragma unroll
          for (int j = 0; j < 2; ++j)
#pragma unroll
            for (int qd = 0; qd < 4; ++qd) {
              const int n = 64 * wc + 32 * j + 8 * qd + 4 * h;
              const f32x4 cj = *(LAS f32x4*)(size_t)(L0 + TAB + 1024 + n * 4);
              const f32x4 a4 = {acc[i][j][4 * qd], acc[i][j][4 * qd + 1], acc[i][j][4 * qd + 2], acc[i][j][4 * qd + 3]};
              const f32x4 v = (a4 - cj * st[0]) * rr;
              const u32x2 o = {pk_bf16(v[0], v[1]), pk_bf16(v[2], v[3])};
              __builtin_amdgcn_raw_buffer_store_b64(o, rO, (u32)((32 * ct + (n >> 2)) * 16384 + (128 * RT + row) * 8), 0, 16);
            }
        }
        STAMP(14);
        WAITV(0);
        STAMP(15);
      }
      BAR();
      if (multi && tid == 0) sy.post(FL_QKV + RT * 15 + ct);
    } else {
      colsum_job(wg, wu, ws, 2 * (4 * (T >> 1) + s - 28), L0 + 96 * 1024, tid, lane, w);
      if (multi && tid == 0) sy.post(FL_CS + 4 * (T >> 1) + s - 28);
    }
  }
  if (w == 7) asm volatile("s_waitcnt vmcnt(0)" : "+v"(pfd));
  STAMP(1);

  if (ph_lo <= 2 && 2 < ph_hi) {
    const int QB = s >> 2, hd = 4 * T + (s & 3);
    constexpr u32 PL = 4128, LK = 81920, LV = LK + 5 * PL;
    if (tid < 512) *(LAS u32x2*)(size_t)(L0 + LV + 5 * PL + tid * 8) = (u32x2){0x00003f80u, 0u};
    if (multi && w == 0) {
      int idx = -1;
      if (lane < 24) {
        const int sec = lane >> 3, r4 = (lane >> 1) & 3, e = lane & 1;
        const int RT = 2 * QB - 2 + r4;
        if (RT >= 0 && !(sec == 0 && r4 < 2)) idx = FL_QKV + RT * 15 + 5 * sec + (20 * hd + 19 * e) / 128;
      }
      sy.wait(idx);
    }
    BAR();
    STAMP(2);
    if (w >= 8) {
      const u32x4 rQ = mk_rsrc(ws + WS_QKV, SZ_QKV);
      if (QB > 0 || lw >= 2) {
#pragma unroll 1
        for (int i = 0; i < 10; ++i) {
          const int tv = i >= 5, j = i - 5 * tv;
          dma_c(rQ, lane * 16, (u32)((160 + 160 * tv + 5 * hd + j) * 16384 + (256 * (QB - 1) + 128 * lw) * 8), L0 + (tv ? LV : LK) + j * PL + lw * 1024);
        }
      }
      WAITV(0);
      BAR();
      if (multi && own35) prefetch_wo();
    } else {
      const int q = lane & 31, h = lane >> 5;
      s16x8 q1; s16x4 q2;
      {
        const __amdgpu_buffer_rsrc_t rQ = mk_rs(ws + WS_QKV, SZ_QKV);
        const u32 tok = (u32)(256 * QB + 32 * w + q) * 8;
        const u32x2 a = __builtin_amdgcn_raw_buffer_load_b64(rQ, (5 * hd + 2 * h) * 16384 + tok, 0, 16);
        const u32x2 c = __builtin_amdgcn_raw_buffer_load_b64(rQ, (5 * hd + 2 * h + 1) * 16384 + tok, 0, 16);
        const u32x2 d = __builtin_amdgcn_raw_buffer_load_b64(rQ, h ? OOB : (5 * hd + 4) * 16384 + tok, 0, 16);
        q1 = cat(__builtin_bit_cast(s16x4, a), __builtin_bit_cast(s16x4, c));
        q2 = __builtin_bit_cast(s16x4, d);
      }
      BAR();
      STAMP(16);
      const u32 kadr = L0 + LK + 2 * h * PL + (32 * w + q) * 8;
      const u32 k2adr = L0 + LK + 4 * PL + (32 * w + q) * 8;
      const u32 vadr = L0 + LV + (4 * (g & 1) + p4) * PL + (32 * w + 4 * h + q4) * 8;
      const f32x16 zero16 = {0, 0, 0, 0, 0, 0, 0, 0, 0, 0, 0, 0, 0, 0, 0, 0};
      const int qd = q - 4 * h;
      constexpr float NEG = -3.0e38f;
      auto score = [&](int j, const f32x16& c) -> f32x16 {
        return mfma32k8(lds_b64(k2adr + j * 256), q2, mfma32(cat(lds_b64(kadr + j * 256), lds_b64(kadr + PL + j * 256)), q1, c));
      };
      auto pv = [&](int j, int ks, u32 p0, u32 p1, u32 p2, u32 p3, const f32x16& o) -> f32x16 {
        const u32 a = vadr + j * 256 + ks * 128;
        return mfma32(cat(lds_tr(a), lds_tr(a + 64)), __builtin_bit_cast(s16x8, (u32x4){p0, p1, p2, p3}), o);
      };
      f32x16 o = zero16;
      float m = NEG;
      const int jstart = QB > 0 ? 1 : (8 - w > 1 ? 8 - w : 1);
      if (QB > 0) {
        const f32x16 sf = score(0, zero16), sd = score(8, zero16);
#pragma unroll
        for (int r = 0; r < 16; r += 2) {
          const float e0 = ((r & 3) + 8 * (r >> 2) <= qd) ? sd[r] : sf[r], e1 = (((r + 1) & 3) + 8 * ((r + 1) >> 2) <= qd) ? sd[r + 1] : sf[r + 1];
          m = fmaxf(fmaxf(m, e0), e1);
        }
#pragma unroll
        for (int j = 1; j < 8; ++j) {
          const f32x16 sc = score(j, zero16);
#pragma unroll
          for (int r = 0; r < 16; r += 2) m = fmaxf(fmaxf(m, sc[r]), sc[r + 1]);
        }
      } else {
        const f32x16 sd = score(8, zero16);
#pragma unroll
        for (int r = 0; r < 16; ++r) m = fmaxf(m, ((r & 3) + 8 * (r >> 2) <= qd) ? sd[r] : NEG);
#pragma unroll 1
        for (int j = jstart; j < 8; ++j) {
          const f32x16 sc = score(j, zero16);
#pragma unroll
          for (int r = 0; r < 16; r += 2) m = fmaxf(fmaxf(m, sc[r]), sc[r + 1]);
        }
      }
      m = xhalf_max(m);
      STAMP(17);
      asm volatile("" ::: "memory");
      f32x16 negm;
#pragma unroll
      for (int r = 0; r < 16; ++r) negm[r] = -m;
      auto full = [&](int j) {
        const f32x16 sc = score(j, negm);
        u32 pk[8];
#pragma unroll
        for (int i = 0; i < 8; ++i) pk[i] = pk_bf16(__builtin_amdgcn_exp2f(sc[2 * i]), __builtin_amdgcn_exp2f(sc[2 * i + 1]));
        o = pv(j, 0, pk[0], pk[1], pk[2], pk[3], o);
        o = pv(j, 1, pk[4], pk[5], pk[6], pk[7], o);
      };
      if (QB > 0) {
#pragma unroll
        for (int j = 1; j < 8; ++j) full(j);
      } else {
#pragma unroll 1
        for (int j = jstart; j < 8; ++j) full(j);
      }
      {
        const f32x16 sd = score(8, negm);
        f32x16 sf;
        if (QB > 0) sf = score(0, negm);
        else {
#pragma unroll
          for (int r = 0; r < 16; ++r) sf[r] = NEG;
        }
        u32 pd[8], pf[8];
#pragma unroll
        for (int i = 0; i < 8; ++i) {
          const bool v0 = ((2 * i) & 3) + 8 * ((2 * i) >> 2) <= qd, v1 = ((2 * i + 1) & 3) + 8 * ((2 * i + 1) >> 2) <= qd;
          const u32 pk = pk_bf16(__builtin_amdgcn_exp2f(v0 ? sd[2 * i] : sf[2 * i]), __builtin_amdgcn_exp2f(v1 ? sd[2 * i + 1] : sf[2 * i + 1]));
          const u32 mk = (v0 ? 0xffffu : 0u) | (v1 ? 0xffff0000u : 0u);
          pd[i] = pk & mk; pf[i] = pk & ~mk;
        }
        o = pv(8, 0, pd[0], pd[1], pd[2], pd[3], o);
        o = pv(8, 1, pd[4], pd[5], pd[6], pd[7], o);
        if (QB > 0) {
          o = pv(0, 0, pf[0], pf[1], pf[2], pf[3], o);
          o = pv(0, 1, pf[4], pf[5], pf[6], pf[7], o);
        }
      }
      STAMP(18);
      auto r = __builtin_amdgcn_permlane32_swap(__float_as_uint(o[8]), __float_as_uint(o[8]), false, false);
      const float inv = 1.0f / __uint_as_float(r[1]);
      const __amdgpu_buffer_rsrc_t rO = mk_rs(ws + WS_O, SZ_O);
      const u32 tok = (u32)(256 * QB + 32 * w + q) * 8;
#pragma unroll
      for (int i = 0; i < 3; ++i) {
        const u32x2 v = {pk_bf16(o[4 * i] * inv, o[4 * i + 1] * inv), pk_bf16(o[4 * i + 2] * inv, o[4 * i + 3] * inv)};
        __builtin_amdgcn_raw_buffer_store_b64(v, rO, (i == 2 && h) ? OOB : (u32)((5 * hd + 2 * i + h) * 16384) + tok, 0, 16);
      }
      WAITV(0);
    }
    BAR();
    if (multi && tid == 0) sy.post(FL_ATT + QB * 32 + hd);
  }
  STAMP(3);

  u32 hres[3][2][2];
  const int wr3 = (w >> 1) & 1, wc3 = w & 1, kh = (w >> 2) & 1;
  u32 nb_adr[2];
  {
    const int sw = ((q4 >> 1) & 1) | ((g & 1) << 1);
#pragma unroll
    for (int j = 0; j < 2; ++j) nb_adr[j] = (8 * g + q4) * 128 + (((2 * wc3 + j) ^ sw) * 32) + 8 * p4;
  }
#define N_MMA(X, W) do { _Pragma("unroll") for (int ks = 0; ks < 2; ++ks) _Pragma("unroll") for (int i = 0; i < 3; ++i) _Pragma("unroll") for (int j = 0; j < 2; ++j) \
      acc[i][j] = mfma16(W[ks][j], X[ks][i], acc[i][j]); } while (0)
#define N_REDUCE(base) do { \
    if (kh) { _Pragma("unroll") for (int i = 0; i < 3; ++i) _Pragma("unroll") for (int j = 0; j < 2; ++j) *(LAS f32x4*)(size_t)((base) + ((w & 3) * 6 + i * 2 + j) * 1024 + lane * 16) = acc[i][j]; } \
    BAR(); \
    if (!kh) { _Pragma("unroll") for (int i = 0; i < 3; ++i) _Pragma("unroll") for (int j = 0; j < 2; ++j) acc[i][j] += *(LAS f32x4*)(size_t)((base) + ((w & 3) * 6 + i * 2 + j) * 1024 + lane * 16); } } while (0)

  if (ph_lo <= 3 && 3 < ph_hi) {
    if (own35) {
      constexpr u32 LA = 81920;
      u32x2 xr[3][2];
      if (w < 4) {
        const __amdgpu_buffer_rsrc_t rX = mk_rs(x, SEQ * DM * 2);
#pragma unroll
        for (int i = 0; i < 3; ++i)
#pragma unroll
          for (int j = 0; j < 2; ++j) {
            const int r = 48 * wr3 + 16 * i + l15;
            xr[i][j] = __builtin_amdgcn_raw_buffer_load_b64(rX, r < nrow3 ? (u32)((row3 + r) * 1280 + (64 * ct3 + 32 * wc3 + 16 * j + 4 * g) * 2) : OOB, 0, 0);
          }
      }
      if (!multi && w >= 8) prefetch_wo();
      if (multi && w == 0) sy.wait(lane < 32 ? FL_ATT + T * 32 + lane : -1);
      BAR();
      STAMP(4);
      if (w >= 8) {
        const u32x4 rO = mk_rsrc(ws + WS_O, SZ_O);
        u32 vA[3];
#pragma unroll
        for (int ii = 0; ii < 3; ++ii) {
          const int u = (lw * 3 + ii) * 64 + lane, pl = u / 48, un = (u - pl * 48) ^ (((pl >> 1) & 1) << 3);
          vA[ii] = 2 * un < nrow3 ? (u32)(pl * 16384 + (row3 + 2 * un) * 8) : OOB;
        }
        auto issue = [&](int c, int slot) {
#pragma unroll
          for (int ii = 0; ii < 3; ++ii) dma_c(rO, vA[ii], c * (16 * 16384), L0 + LA + slot * 12288 + (lw * 3 + ii) * 1024);
        };
#pragma unroll 1
        for (int c = 0; c < 6; ++c) issue(c, c);
        WAITV(12); BAR();
        WAITV(6);
#pragma unroll 1
        for (int i = 0; i < 5; ++i) {
          BAR();
          if (i < 2) { issue(2 * i + 6, 2 * i); issue(2 * i + 7, 2 * i + 1); WAITV(6); } else WAITV(0);
        }
        BAR();
      } else {
        f32x4 acc[3][2];
#pragma unroll
        for (int i = 0; i < 3; ++i) { acc[i][0] = (f32x4){0, 0, 0, 0}; acc[i][1] = (f32x4){0, 0, 0, 0}; }
        u32 aadr[3];
#pragma unroll
        for (int i = 0; i < 3; ++i) {
          const int tok = 48 * wr3 + 16 * i + l15;
          aadr[i] = LA + kh * 12288 + (2 * g) * 768 + (((tok >> 1) ^ ((g & 1) << 3)) * 16) + (tok & 1) * 8;
        }
        s16x8 xA[2][3], wA[2][2], xB[2][3], wB[2][2];
#define P3_READ(X, W, sa, sb) do { _Pragma("unroll") for (int ks = 0; ks < 2; ++ks) { \
          _Pragma("unroll") for (int j = 0; j < 2; ++j) { const u32 a_ = (sb) + nb_adr[j] + ks * 4096; W[ks][j] = cat(lds_tr(a_), lds_tr(a_ + 512)); } \
          _Pragma("unroll") for (int i = 0; i < 3; ++i) { const u32 a_ = (sa) + aadr[i] + ks * 8 * 768; X[ks][i] = cat(lds_b64(a_), lds_b64(a_ + 768)); } } } while (0)
#define P3_SCHED() do { _Pragma("unroll") for (int z = 0; z < 10; ++z) { SGB(M_DSR, 2); SGB(M_MFMA, 1); } SGB(M_MFMA, 2); SCHED0(); } while (0)
        const u32 wb = L0 + kh * 8192;
        BAR();
        P3_READ(xA, wA, L0, wb);
#pragma unroll 1
        for (int st = 0; st < 4; st += 2) {
          BAR(); SCHED0();
          P3_READ(xB, wB, L0 + (st == 0 ? 1 : 0) * 24576, wb + (st + 1) * 16384);
          N_MMA(xA, wA);
          P3_SCHED();
          BAR(); SCHED0();
          P3_READ(xA, wA, L0 + (st == 0 ? 2 : 1) * 24576, wb + (st + 2) * 16384);
          N_MMA(xB, wB);
          P3_SCHED();
        }
        BAR();
        N_MMA(xA, wA);
        N_REDUCE(L0);
        STAMP(19);
        if (w < 4) {
          const __amdgpu_buffer_rsrc_t rH = mk_rs(ws + WS_H, SZ_H);
#pragma unroll
          for (int i = 0; i < 3; ++i) {
            float hv[8];
#pragma unroll
            for (int j = 0; j < 2; ++j) {
              const u32 a0 = pk_bf16(acc[i][j][0], acc[i][j][1]), a1 = pk_bf16(acc[i][j][2], acc[i][j][3]);
              hres[i][j][0] = pk_bf16(bf_lo(xr[i][j][0]) + bf_lo(a0), bf_hi(xr[i][j][0]) + bf_hi(a0));
              hres[i][j][1] = pk_bf16(bf_lo(xr[i][j][1]) + bf_lo(a1), bf_hi(xr[i][j][1]) + bf_hi(a1));
              hv[4 * j] = bf_lo(hres[i][j][0]); hv[4 * j + 1] = bf_hi(hres[i][j][0]); hv[4 * j + 2] = bf_lo(hres[i][j][1]); hv[4 * j + 3] = bf_hi(hres[i][j][1]);
            }
            auto s0 = __builtin_amdgcn_permlane16_swap(hres[i][0][0], hres[i][1][0], false, false);
            auto s1 = __builtin_amdgcn_permlane16_swap(hres[i][0][1], hres[i][1][1], false, false);
            const int r = 48 * wr3 + 16 * i + l15;
            const int oct = 8 * ct3 + 4 * wc3 + ((g & 1) << 1) + (g >> 1);
            __builtin_amdgcn_raw_buffer_store_b128((u32x4){s0[0], s1[0], s0[1], s1[1]}, rH, r < nrow3 ? (u32)(oct * 32768 + (row3 + r) * 16) : OOB, 0, 16);
            float sm = 0.f;
#pragma unroll
            for (int e = 0; e < 8; ++e) sm += hv[e];
            const float mw = sum_x16_x32(sm) * (1.0f / 32.0f);
            float m2 = 0.f;
#pragma unroll
            for (int e = 0; e < 8; ++e) { const float d = hv[e] - mw; m2 += d * d; }
            m2 = sum_x16_x32(m2);
            if (g == 0) *(LAS f32x2*)(size_t)(L0 + L_TAB + (r * 2 + wc3) * 8) = (f32x2){mw, m2};
          }
        }
      }
      BAR();
      if (tid < nrow3) {
        const f32x4 p = *(LAS f32x4*)(size_t)(L0 + L_TAB + tid * 16);
        const float mt = 0.5f * (p[0] + p[2]), d0 = p[0] - mt, d1 = p[2] - mt;
        const float m2 = p[1] + p[3] + 32.0f * (d0 * d0 + d1 * d1);
        __hip_atomic_store((GAS u64*)(ws + WS_HST) + (T * 10 + ct3) * 256 + 96 * rt3 + tid, ((u64)__float_as_uint(m2) << 32) | __float_as_uint(mt), __ATOMIC_RELAXED, __HIP_MEMORY_SCOPE_AGENT);
      }
      if (w < 4) WAITV(0);
      BAR();
      if (multi && tid == 0) sy.post(FL_H + T * 30 + s);
    } else {
      const int job = 16 + 2 * T + s - 30;
      if (job < 27) {
        colsum_job(wg, wu, ws, 2 * job, L0 + 96 * 1024, tid, lane, w);
        if (multi && tid == 0) sy.post(FL_CS + job);
      }
    }
  }
  STAMP(5);

  if (ph_lo <= 4 && 4 < ph_hi) {
    const int R4 = 4 * (T & 1) + (s & 3), c4 = 7 * (T >> 1) + (s >> 2);
    if (s < 28 && c4 < 27) {
      constexpr u32 ST = 24576;
      if (w >= 8) {
        const u32x4 rA = mk_rsrc(ws + WS_H, SZ_H), rG = mk_rsrc(wg, DM * FF * 2), rU = mk_rsrc(wu, DM * FF * 2);
        const u32 vN = [&] { const int k = 8 * lw + (lane >> 3); return (u32)(k * (FF * 2) + c4 * 128 + ((((lane & 7) >> 2) ^ ((k >> 1) & 1)) * 64) + (lane & 3) * 16); }();
        auto issueB = [&](int t, int slot) {
          dma(rG, vN, t * (32 * FF * 2), L0 + slot * ST + 16384 + lw * 1024);
          dma(rU, vN, t * (32 * FF * 2), L0 + slot * ST + 20480 + lw * 1024);
        };
        const u32 vA = (u32)((256 * R4 + lane) * 16);
        auto issueA = [&](int t, int slot) {
#pragma unroll
          for (int ii = 0; ii < 4; ++ii) dma_c(rA, vA, (4 * t + lw) * 32768 + ii * 1024, L0 + slot * ST + lw * 4096 + ii * 1024);
        };
#pragma unroll 1
        for (int t = 0; t < 5; ++t) issueB(t, t);
        BAR();
#pragma unroll 1
        for (int t = 0; t < 5; ++t) issueA(t, t);
        WAITV(16); BAR();
        WAITV(12);
        int slot = 5;
#pragma unroll 1
        for (int t = 0; t < 15; ++t) {
          BAR();
          issueA(t + 5, slot); issueB(t + 5, slot); slot = slot == 5 ? 0 : slot + 1;
          if (t < 2) WAITV(14); else WAITV(18);
        }
        asm volatile("" ::: "memory");
        const int row = tid - 512;
        const __amdgpu_buffer_rsrc_t rS = mk_rs(ws + WS_HST, SZ_HST), rC = mk_rs(ws + WS_CS, SZ_CS);
        u32x2 pp[10];
#pragma unroll
        for (int i = 0; i < 10; ++i) pp[i] = __builtin_amdgcn_raw_buffer_load_b64(rS, (u32)(((R4 * 10 + i) * 256 + row) * 8), 0, 16);
        const u32 csv = __builtin_amdgcn_raw_buffer_load_b32(rC, (u32)((((row >> 6) & 1) * 27 + c4) * 256 + (row & 63) * 4), 0, 16);
        BAR(); WAITV(23);
        BAR(); WAITV(17);
        BAR(); WAITV(0);
        {
          float sm = 0.f;
#pragma unroll
          for (int i = 0; i < 10; ++i) sm += __uint_as_float(pp[i][0]);
          const float mean = sm * 0.1f;
          float m2 = 0.f;
#pragma unroll
          for (int i = 0; i < 10; ++i) { const float d = __uint_as_float(pp[i][0]) - mean; m2 += __uint_as_float(pp[i][1]) + 64.0f * d * d; }
          float m, r; ln_stats(mean, m2 * (1.0f / DM), m, r);
          *(LAS f32x2*)(size_t)(L0 + L_TAB + row * 8) = (f32x2){m, r};
          if (row < 128) *(LAS u32*)(size_t)(L0 + L_TAB + 2048 + row * 4) = csv;
        }
        BAR(); BAR();
        BAR();
      } else {
        if (multi && w == 0) {
          int idx = -1;
          if (lane < 30) idx = FL_H + R4 * 30 + lane;
          else if (lane == 30) idx = FL_CS + (c4 >> 1);
          else if (lane == 31) idx = FL_CS + ((27 + c4) >> 1);
          sy.wait(idx);
        }
        BAR();
        STAMP(6);
        const int wr = w >> 1, wc = w & 1, r31 = lane & 31, h = lane >> 5, blk = g & 1;
        f32x16 ag[2], au[2];
#pragma unroll
        for (int i = 0; i < 2; ++i)
#pragma unroll
          for (int r = 0; r < 16; ++r) { ag[i][r] = 0.f; au[i][r] = 0.f; }
        const u32 xadr = h * 4096 + (64 * wr + r31) * 16;
        const u32 wadr = 16384 + (8 * h + q4) * 128 + ((wc ^ ((q4 >> 1) & 1)) * 64) + blk * 32 + 8 * p4;
        s16x8 xA[2][2], gA[2], uA[2], xB[2][2], gB[2], uB[2];
#define P4_READ(X, G_, U_, base) do { _Pragma("unroll") for (int kk = 0; kk < 2; ++kk) { const u32 a_ = (base) + wadr + kk * 2048; \
          G_[kk] = cat(lds_tr(a_), lds_tr(a_ + 512)); U_[kk] = cat(lds_tr(a_ + 4096), lds_tr(a_ + 4096 + 512)); \
          _Pragma("unroll") for (int i = 0; i < 2; ++i) X[kk][i] = lds_b128((base) + xadr + kk * 8192 + i * 512); } } while (0)
#define P4_MMA(X, G_, U_) do { _Pragma("unroll") for (int kk = 0; kk < 2; ++kk) _Pragma("unroll") for (int i = 0; i < 2; ++i) { \
          ag[i] = mfma32(G_[kk], X[kk][i], ag[i]); au[i] = mfma32(U_[kk], X[kk][i], au[i]); } } while (0)
#define P4_SCHED() do { _Pragma("unroll") for (int z = 0; z < 6; ++z) { SGB(M_MFMA, 1); SGB(M_DSR, 2); } SGB(M_MFMA, 2); SCHED0(); } while (0)
        BAR();
        STAMP(20);
        CSTAMP(26);
        P4_READ(xA, gA, uA, L0);
        u32 base = L0 + ST;
#pragma unroll 1
        for (int t = 0; t < 20; t += 2) {
          PBAR(); SCHED0();
          P4_READ(xB, gB, uB, base);
          P4_MMA(xA, gA, uA);
          P4_SCHED();
          base = base == L0 + 5 * ST ? L0 : base + ST;
          PBAR(); SCHED0();
          P4_READ(xA, gA, uA, base);
          P4_MMA(xB, gB, uB);
          P4_SCHED();
          base = base == L0 + 5 * ST ? L0 : base + ST;
        }
        BAR();
        STAMP(21);
        CSTAMP(27);
        const __amdgpu_buffer_rsrc_t rH = mk_rs(ws + WS_G, SZ_G);
#pragma unroll
        for (int i = 0; i < 2; ++i) {
          const int row = 64 * wr + 32 * i + r31;
          const f32x2 st = *(LAS f32x2*)(size_t)(L0 + L_TAB + row * 8);
          const float r2 = st[1] * st[1];
          u32 pk[4][2];
#pragma unroll
          for (int qd = 0; qd < 4; ++qd) {
            const int n = 32 * wc + 8 * qd + 4 * h;
            const f32x4 cg = *(LAS f32x4*)(size_t)(L0 + L_TAB + 2048 + n * 4), cu = *(LAS f32x4*)(size_t)(L0 + L_TAB + 2048 + 256 + n * 4);
            const f32x4 g4 = {ag[i][4 * qd], ag[i][4 * qd + 1], ag[i][4 * qd + 2], ag[i][4 * qd + 3]}, u4 = {au[i][4 * qd], au[i][4 * qd + 1], au[i][4 * qd + 2], au[i][4 * qd + 3]};
            f32x4 a = g4 - cg * st[0];
            const f32x4 u = (u4 - cu * st[0]) * r2;
#pragma unroll
            for (int e = 0; e < 4; ++e) a[e] = fmaxf(a[e], 0.f) * u[e];
            pk[qd][0] = pk_bf16(a[0], a[1]); pk[qd][1] = pk_bf16(a[2], a[3]);
          }
#pragma unroll
          for (int o = 0; o < 4; o += 2) {
            auto s0 = __builtin_amdgcn_permlane32_swap(pk[o][0], pk[o + 1][0], false, false);
            auto s1 = __builtin_amdgcn_permlane32_swap(pk[o][1], pk[o + 1][1], false, false);
            __builtin_amdgcn_raw_buffer_store_b128((u32x4){s0[0], s1[0], s0[1], s1[1]}, rH, (u32)((8 * c4 + 4 * wc + o + h) * 32768 + (256 * R4 + row) * 16), 0, 16);
          }
        }
        STAMP(22);
        WAITV(0);
      }
      BAR();
      if (multi && tid == 0) sy.post(FL_FF + R4 * 27 + c4);
    } else if (multi && s >= 28 && w >= 8) {
      const u32x4 rW = mk_rsrc(wd, FF * DM * 2);
#pragma unroll 1
      for (int i = 0; i < 135; ++i) {
        dma(rW, lane * 16, (u32)(((s - 28) * 540 + lw * 135 + i) * 1024), L0 + (lw * 16 + (i & 15)) * 1024);
        WAITV(24);
      }
      WAITV(0);
    }
  }
  STAMP(7);

  if (ph_lo <= 5 && 5 < ph_hi) {
    if (own35) {
      constexpr u32 LA = 81920;
      if (w >= 8) {
        const u32x4 rW = mk_rsrc(wd, FF * DM * 2), rG = mk_rsrc(ws + WS_G, SZ_G);
        const u32 vB0 = bn_voff(2 * lw, 1280, ct3 * 128), vB1 = bn_voff(2 * lw + 1, 1280, ct3 * 128);
        auto issueB = [&](int i, int slot) {
#pragma unroll
          for (int cc = 0; cc < 2; ++cc) {
            const int c = 2 * i + cc > 26 ? 26 : 2 * i + cc;
            dma(rW, vB0, c * (64 * 1280), L0 + slot * 16384 + cc * 8192 + lw * 2048);
            dma(rW, vB1, c * (64 * 1280), L0 + slot * 16384 + cc * 8192 + lw * 2048 + 1024);
          }
        };
#pragma unroll 1
        for (int i = 0; i < 5; ++i) issueB(i, i);
        u32 vA[3];
#pragma unroll
        for (int ii = 0; ii < 3; ++ii) {
          const int u = (lw * 3 + ii) * 64 + lane, pl = u / 96, tk = u - pl * 96;
          vA[ii] = tk < nrow3 ? (u32)(pl * 32768 + (row3 + tk) * 16) : OOB;
        }
        auto issueA = [&](int i, int slot) {
#pragma unroll
          for (int cc = 0; cc < 2; ++cc) {
            const int c = 2 * i + cc > 26 ? 26 : 2 * i + cc;
#pragma unroll
            for (int ii = 0; ii < 3; ++ii) dma_c(rG, vA[ii], c * (8 * 32768), L0 + LA + slot * 24576 + cc * 12288 + (lw * 3 + ii) * 1024);
          }
        };
        BAR();
#pragma unroll 1
        for (int i = 0; i < 3; ++i) issueA(i, i);
        WAITV(12); BAR();
        WAITV(6);
        int sa = 0, sb = 0;
#pragma unroll 1
        for (int i = 0; i < 14; ++i) {
          BAR();
          if (i <= 10) { issueA(i + 3, sa); sa = sa == 2 ? 0 : sa + 1; }
          if (i <= 8) { issueB(i + 5, sb); sb = sb == 4 ? 0 : sb + 1; }
          if (i >= 1 && i <= 8) WAITV(14); else if (i == 0 || i == 9) WAITV(10); else if (i == 10) WAITV(6); else WAITV(0);
        }
        BAR();
      } else {
        if (!multi && w < 4) {
          const __amdgpu_buffer_rsrc_t rH = mk_rs(ws + WS_H, SZ_H);
#pragma unroll
          for (int i = 0; i < 3; ++i)
#pragma unroll
            for (int j = 0; j < 2; ++j) {
              const int r = 48 * wr3 + 16 * i + l15, col = 64 * ct3 + 32 * wc3 + 16 * j + 4 * g;
              const u32x2 v = __builtin_amdgcn_raw_buffer_load_b64(rH, r < nrow3 ? (u32)((col >> 3) * 32768 + (row3 + r) * 16 + (col & 7) * 2) : OOB, 0, 0);
              hres[i][j][0] = v[0]; hres[i][j][1] = v[1];
            }
        }
        if (multi && w == 0) sy.wait(lane < 27 ? FL_FF + T * 27 + lane : -1);
        BAR();
        STAMP(8);
        f32x4 acc[3][2];
#pragma unroll
        for (int i = 0; i < 3; ++i) { acc[i][0] = (f32x4){0, 0, 0, 0}; acc[i][1] = (f32x4){0, 0, 0, 0}; }
        const u32 aadr = LA + kh * 12288 + g * 1536 + (48 * wr3 + l15) * 16;
        s16x8 xA[2][3], wA[2][2], xB[2][3], wB[2][2];
#define P5_READ(X, W, sa, sb) do { _Pragma("unroll") for (int ks = 0; ks < 2; ++ks) { \
          _Pragma("unroll") for (int j = 0; j < 2; ++j) { const u32 a_ = (sb) + nb_adr[j] + ks * 4096; W[ks][j] = cat(lds_tr(a_), lds_tr(a_ + 512)); } \
          _Pragma("unroll") for (int i = 0; i < 3; ++i) X[ks][i] = lds_b128((sa) + aadr + ks * 4 * 1536 + i * 256); } } while (0)
#define P5_SCHED() do { _Pragma("unroll") for (int z = 0; z < 7; ++z) { SGB(M_DSR, 2); SGB(M_MFMA, 1); } SGB(M_MFMA, 5); SCHED0(); } while (0)
        BAR();
        P5_READ(xA, wA, L0, L0 + kh * 8192);
        u32 pa = L0 + 24576, pb = L0 + kh * 8192 + 16384;
        const u32 pa_end = L0 + 2 * 24576, pb_end = L0 + kh * 8192 + 4 * 16384;
#pragma unroll 1
        for (int i = 0; i < 12; i += 2) {
          BAR(); SCHED0();
          P5_READ(xB, wB, pa, pb);
          N_MMA(xA, wA);
          P5_SCHED();
          pa = pa == pa_end ? L0 : pa + 24576; pb = pb == pb_end ? L0 + kh * 8192 : pb + 16384;
          BAR(); SCHED0();
          P5_READ(xA, wA, pa, pb);
          N_MMA(xB, wB);
          P5_SCHED();
          pa = pa == pa_end ? L0 : pa + 24576; pb = pb == pb_end ? L0 + kh * 8192 : pb + 16384;
        }
        BAR(); SCHED0();
        P5_READ(xB, wB, pa, pb);
        N_MMA(xA, wA);
        P5_SCHED();
        BAR();
        if (!kh) N_MMA(xB, wB);
        N_REDUCE(L0);
        STAMP(23);
        if (w < 4) {
#pragma unroll
          for (int i = 0; i < 3; ++i) {
            u32 pk[2][2];
#pragma unroll
            for (int j = 0; j < 2; ++j) {
              const u32 a0 = pk_bf16(acc[i][j][0], acc[i][j][1]), a1 = pk_bf16(acc[i][j][2], acc[i][j][3]);
              pk[j][0] = pk_bf16(bf_lo(hres[i][j][0]) + bf_lo(a0), bf_hi(hres[i][j][0]) + bf_hi(a0));
              pk[j][1] = pk_bf16(bf_lo(hres[i][j][1]) + bf_lo(a1), bf_hi(hres[i][j][1]) + bf_hi(a1));
            }
            auto s0 = __builtin_amdgcn_permlane16_swap(pk[0][0], pk[1][0], false, false);
            auto s1 = __builtin_amdgcn_permlane16_swap(pk[0][1], pk[1][1], false, false);
            const int r = 48 * wr3 + 16 * i + l15;
            const int oct = 8 * ct3 + 4 * wc3 + ((g & 1) << 1) + (g >> 1);
            if (r < nrow3) *(u32x4*)(out + (size_t)(row3 + r) * DM + oct * 8) = (u32x4){s0[0], s1[0], s0[1], s1[1]};
          }
        }
      }
    }
    if (multi && b == 0 && w == 0) {
#pragma unroll 1
      for (int i = 0; i < 4; ++i) sy.wait(FL_ATT + i * 64 + lane);
      if (lane == 0) __hip_atomic_store((GAS u64*)(ws + WS_EPOCH), sy.tag, __ATOMIC_RELAXED, __HIP_MEMORY_SCOPE_AGENT);
    }
  }
  STAMP(9);
}

extern "C" void kernel_launch(void* const* d_in, const int* in_sizes, int n_in, void* d_out, int out_size, void* d_ws, size_t ws_size, hipStream_t stream) {
  (void)in_sizes; (void)n_in; (void)out_size; (void)ws_size;
  const u16* x = (const u16*)d_in[0]; const u16* wqkv = (const u16*)d_in[1]; const u16* wo = (const u16*)d_in[2];
  const u16* wg = (const u16*)d_in[4]; const u16* wu = (const u16*)d_in[5]; const u16* wd = (const u16*)d_in[6];
  (void)hipFuncSetAttribute((const void*)fused_block, hipFuncAttributeMaxDynamicSharedMemorySize, LDS_BYTES);
  fused_block<<<256, NTHR, LDS_BYTES, stream>>>(x, wqkv, wo, wg, wu, wd, (u16*)d_out, (char*)d_ws, 1, 6);
}
```

```cpp
#include <hip/hip_runtime.h>
#include <stdint.h>

typedef unsigned short u16;
typedef unsigned int u32;
typedef unsigned long long u64;
typedef short s16x4 __attribute__((ext_vector_type(4)));
typedef short s16x8 __attribute__((ext_vector_type(8)));
typedef __bf16 bf16x8 __attribute__((ext_vector_type(8)));
typedef __bf16 bf16x2 __attribute__((ext_vector_type(2)));
typedef float f32x2 __attribute__((ext_vector_type(2)));
typedef float f32x4 __attribute__((ext_vector_type(4)));
typedef float f32x16 __attribute__((ext_vector_type(16)));
typedef u32 u32x2 __attribute__((ext_vector_type(2)));
typedef u32 u32x4 __attribute__((ext_vector_type(4)));
#define LAS __attribute__((address_space(3)))
#define GAS __attribute__((address_space(1)))
#define DEV __device__ __forceinline__

#define NLAUNCH 1
#define STAMPS 0

constexpr int SEQ = 2048, DM = 640, FF = 1728;
constexpr u32 WS_EPOCH = 0;
constexpr u32 WS_FLAGS = 4096;
constexpr u32 FL_QKV = 0, FL_ATT = 256, FL_H = 512, FL_FF = 768, FL_CS = 1000;
constexpr u32 WS_QKV = 256 * 1024;
constexpr u32 SZ_QKV = 240 * 32768;
constexpr u32 WS_O = WS_QKV + SZ_QKV;
constexpr u32 SZ_O = 160 * 16384;
constexpr u32 WS_H = WS_O + SZ_O;
constexpr u32 SZ_H = 80 * 32768;
constexpr u32 WS_HST = WS_H + SZ_H;
constexpr u32 SZ_HST = 8 * 20 * 256 * 8;
constexpr u32 WS_G = WS_HST + SZ_HST;
constexpr u32 SZ_G = 216 * 32768;
constexpr u32 WS_CS = WS_G + SZ_G;
constexpr u32 SZ_CS = 54 * 64 * 4;
constexpr u32 WS_STAMP = WS_CS + 16384;
constexpr u32 LDS_BYTES = 160 * 1024;
constexpr u32 L_TAB = 156 * 1024;
constexpr int NTHR = 768;
constexpr u32 OOB = 0x80000000u;
constexpr int CODE_KB = 30;

DEV u32x4 mk_rsrc(const void* p, u32 bytes) {
  const u64 a = (u64)p;
  u32x4 r; r[0] = (u32)a; r[1] = (u32)(a >> 32) & 0xffffu; r[2] = bytes; r[3] = 0x00020000u; return r;
}
DEV __amdgpu_buffer_rsrc_t mk_rs(const void* p, u32 bytes) { return __builtin_amdgcn_make_buffer_rsrc((void*)p, 0, bytes, 0x00020000); }
DEV void dma(u32x4 rs, u32 voff, u32 soff, u32 lds) {
  asm volatile("s_mov_b32 m0, %0\n\ts_nop 0\n\tbuffer_load_dwordx4 %1, %2, %3 offen lds" ::"s"(lds), "v"(voff), "s"(rs), "s"(soff));
}
DEV void dma_c(u32x4 rs, u32 voff, u32 soff, u32 lds) {
  asm volatile("s_mov_b32 m0, %0\n\ts_nop 0\n\tbuffer_load_dwordx4 %1, %2, %3 offen sc1 lds" ::"s"(lds), "v"(voff), "s"(rs), "s"(soff));
}
#define WAITV(n) asm volatile("s_waitcnt vmcnt(" #n ")" ::: "memory")
#define BAR() asm volatile("s_waitcnt lgkmcnt(0)\n\ts_barrier" ::: "memory")
DEV u32 pk_bf16(float lo, float hi) { f32x2 v = {lo, hi}; return __builtin_bit_cast(u32, __builtin_convertvector(v, bf16x2)); }
DEV float bf_lo(u32 d) { return __uint_as_float(d << 16); }
DEV float bf_hi(u32 d) { return __uint_as_float(d & 0xffff0000u); }
DEV float rbf(float x) { return bf_lo(pk_bf16(x, x)); }
DEV s16x8 lds_b128(u32 a) { return *(LAS s16x8*)(size_t)a; }
DEV s16x4 lds_b64(u32 a) { return *(LAS s16x4*)(size_t)a; }
DEV s16x4 lds_tr(u32 a) { return __builtin_amdgcn_ds_read_tr16_b64_v4i16((LAS s16x4*)(size_t)a); }
DEV s16x8 cat(s16x4 a, s16x4 b) { return __builtin_shufflevector(a, b, 0, 1, 2, 3, 4, 5, 6, 7); }
DEV f32x4 mfma16(s16x8 a, s16x8 b, f32x4 c) { return __builtin_amdgcn_mfma_f32_16x16x32_bf16(__builtin_bit_cast(bf16x8, a), __builtin_bit_cast(bf16x8, b), c, 0, 0, 0); }
DEV f32x16 mfma32(s16x8 a, s16x8 b, f32x16 c) { return __builtin_amdgcn_mfma_f32_32x32x16_bf16(__builtin_bit_cast(bf16x8, a), __builtin_bit_cast(bf16x8, b), c, 0, 0, 0); }
DEV f32x16 mfma32k8(s16x4 a, s16x4 b, f32x16 c) { return __builtin_amdgcn_mfma_f32_32x32x8bf16_1k(a, b, c, 0, 0, 0); }
DEV float dot2(u32 a, u32 b, float c) { return __builtin_amdgcn_fdot2_f32_bf16(__builtin_bit_cast(bf16x2, a), __builtin_bit_cast(bf16x2, b), c, false); }
DEV float xhalf_max(float m) { auto r = __builtin_amdgcn_permlane32_swap(__float_as_uint(m), __float_as_uint(m), false, false); return fmaxf(__uint_as_float(r[0]), __uint_as_float(r[1])); }
DEV float sum_x16_x32(float s) { s += __shfl_xor(s, 16); s += __shfl_xor(s, 32); return s; }
DEV void ln_stats(float mean, float var, float& m_out, float& r_out) {
  m_out = rbf(mean);
  const float sd = rbf(__builtin_amdgcn_sqrtf(rbf(rbf(var) + 1e-5f)));
  r_out = __builtin_amdgcn_rcpf(sd);
}

struct Sync {
  GAS u64* flags; u64 tag;
  DEV void wait(int idx) const {
    GAS u64* p = flags + (idx < 0 ? 0 : idx) * 16;
    for (u32 spins = 0; spins < (1u << 16); ++spins) {
      const u64 v = __hip_atomic_load(p, __ATOMIC_RELAXED, __HIP_MEMORY_SCOPE_AGENT);
      if (__all(idx < 0 || v == tag)) break;
    }
  }
  DEV void post(int idx) const { __hip_atomic_store(flags + idx * 16, tag, __ATOMIC_RELAXED, __HIP_MEMORY_SCOPE_AGENT); }
};

#define CSTAMP(i) do {} while (0)
#define STAMP(i) do {} while (0)
#define STAMPW(i, wv) do {} while (0)

DEV void colsum_job(const u16* wg, const u16* wu, char* ws, int G0, u32 lds0, int tid, int lane, int w) {
  if (w < 8) {
  u32x4 v[2][10];
#pragma unroll
  for (int sg = 0; sg < 2; ++sg) {
    int G = G0 + sg; if (G > 53) G = 53;
    const u16* W = (G < 27 ? wg + G * 64 : wu + (G - 27) * 64) + (lane & 7) * 8 + (size_t)(8 * w + (lane >> 3)) * FF;
#pragma unroll
    for (int i = 0; i < 10; ++i) v[sg][i] = *(const u32x4*)(W + (size_t)i * 64 * FF);
  }
#pragma unroll
  for (int sg = 0; sg < 2; ++sg) {
    float a[8];
#pragma unroll
    for (int e = 0; e < 8; ++e) a[e] = 0.f;
#pragma unroll
    for (int i = 0; i < 10; ++i)
#pragma unroll
      for (int e = 0; e < 4; ++e) { a[2 * e] += bf_lo(v[sg][i][e]); a[2 * e + 1] += bf_hi(v[sg][i][e]); }
#pragma unroll
    for (int e = 0; e < 8; ++e) { float s = a[e]; s += __shfl_xor(s, 8); s = sum_x16_x32(s); a[e] = s; }
    if (lane < 8) {
      LAS f32x4* p = (LAS f32x4*)(size_t)(lds0 + ((sg * 8 + w) * 64 + lane * 8) * 4);
      p[0] = (f32x4){a[0], a[1], a[2], a[3]}; p[1] = (f32x4){a[4], a[5], a[6], a[7]};
    }
  }
  }
  BAR();
  if (tid < 128) {
    const int sg = tid >> 6, c = tid & 63; float s = 0.f;
#pragma unroll
    for (int ww = 0; ww < 8; ++ww) s += *(LAS float*)(size_t)(lds0 + ((sg * 8 + ww) * 64 + c) * 4);
    if (G0 + sg < 54) __hip_atomic_store((GAS float*)(ws + WS_CS) + (G0 + sg) * 64 + c, s, __ATOMIC_RELAXED, __HIP_MEMORY_SCOPE_AGENT);
  }
  WAITV(0);
  BAR();
}

#define SCHED0() __builtin_amdgcn_sched_barrier(0)
#define SGB(mask, n) __builtin_amdgcn_sched_group_barrier(mask, n, 0)
#define PBAR() asm volatile("s_barrier" ::: "memory")
constexpr int M_MFMA = 0x8, M_DSR = 0x100, M_VALU = 0x2;

__global__ __launch_bounds__(768, 3) void fused_block(const u16* __restrict__ x, const u16* __restrict__ wqkv, const u16* __restrict__ wo,
                                                      const u16* __restrict__ wg, const u16* __restrict__ wu, const u16* __restrict__ wd,
                                                      u16* __restrict__ out, char* __restrict__ ws, int ph_lo, int ph_hi) {
  extern __shared__ __attribute__((aligned(16))) char smem[];
  const u32 L0 = (u32)(size_t)(LAS char*)smem;
  const int tid = threadIdx.x, lane = tid & 63;
  const int w = __builtin_amdgcn_readfirstlane(tid >> 6);
  const int lw = w - 8;
  const int b = blockIdx.x, T = b & 7, s = b >> 3;
  const int l15 = lane & 15, g = lane >> 4, q4 = l15 >> 2, p4 = lane & 3;
  Sync sy; sy.flags = (GAS u64*)(ws + WS_FLAGS); sy.tag = 0;
  const bool multi = (NLAUNCH == 1);
  if (multi && w == 0) {
    const u64 e = __hip_atomic_load((GAS u64*)(ws + WS_EPOCH), __ATOMIC_RELAXED, __HIP_MEMORY_SCOPE_AGENT) + 1;
    sy.tag = ((u64)(u32)__builtin_amdgcn_readfirstlane((u32)(e >> 32)) << 32) | (u32)__builtin_amdgcn_readfirstlane((u32)e);
  }
  u32x4 pfd = {0, 0, 0, 0};
  if (s == 0 && w == 7) {
    u64 pc; asm volatile("s_getpc_b64 %0" : "=s"(pc));
    const char* p = (const char*)(pc & ~127ull) + lane * 16;
#pragma unroll 1
    for (int i = 0; i < CODE_KB; ++i) asm volatile("global_load_dwordx4 %0, %1, off" : "+v"(pfd) : "v"(p + i * 1024));
  }
  STAMP(0);
  const bool own35 = s < 30;
  const int rt3 = s / 10, ct3 = s - rt3 * 10;
  const int nrow3 = rt3 < 2 ? 96 : 64;
  const int row3 = 256 * T + 96 * rt3;

  auto bn_voff = [&](int i8, int pitch, int colb) -> u32 {
    const int k = 8 * i8 + (lane >> 3);
    const int sw = ((k >> 1) & 1) | (((k >> 3) & 1) << 1);
    return (u32)(k * pitch + colb + ((((lane & 7) >> 1) ^ sw) * 32) + (lane & 1) * 16);
  };
  auto prefetch_wo = [&]() {
    const u32x4 rW = mk_rsrc(wo, DM * DM * 2);
    const u32 v0 = bn_voff(0, 1280, ct3 * 128), v1 = bn_voff(1, 1280, ct3 * 128);
#pragma unroll 1
    for (int i = lw * 20; i < lw * 20 + 20; i += 2) {
      dma(rW, v0, i * 8 * 1280, L0 + i * 1024);
      dma(rW, v1, i * 8 * 1280, L0 + i * 1024 + 1024);
    }
  };

  if (ph_lo <= 1 && 1 < ph_hi) {
    if (!(T & 1) || s < 28) {
      const int RT = 4 * (T >> 1) + (s & 3), ct = 8 * (T & 1) + (s >> 2);
      constexpr u32 TAB = 120 * 1024;
      if (w >= 8) {
        const u32x4 rA = mk_rsrc(x, SEQ * DM * 2), rB = mk_rsrc(wqkv, DM * 3 * DM * 2);
        u32 vA[4], vB[4];
#pragma unroll
        for (int ii = 0; ii < 4; ++ii) {
          const int i = lw * 4 + ii;
          const int row = 8 * i + (lane >> 3), c = (lane & 7) ^ ((row >> 1) & 7);
          vA[ii] = (u32)((128 * RT + row) * 1280 + c * 16);
          const int k = 4 * i + g;
          vB[ii] = (u32)(k * 3840 + ct * 256 + (((l15 >> 2) ^ (k & 3)) * 64) + (l15 & 3) * 16);
        }
        auto issue = [&](int c, int slot) {
          const u32 base = L0 + slot * 32768 + lw * 4096;
#pragma unroll
          for (int ii = 0; ii < 4; ++ii) dma(rA, vA[ii], c * 128, base + ii * 1024);
#pragma unroll
          for (int ii = 0; ii < 4; ++ii) dma(rB, vB[ii], c * (64 * 3840), base + 16384 + ii * 1024);
        };
        issue(0, 0); issue(1, 1);
        STAMPW(10, 8);
        WAITV(8); BAR();
        STAMPW(11, 8);
        issue(2, 2);
        WAITV(8);
        int slot = 4;
#pragma unroll 1
        for (int c = 0; c < 10; ++c) {
          BAR();
          if (c == 0) issue(3, 3);
          if (c < 6) { issue(c + 4, slot); slot = slot == 4 ? 0 : slot + 1; WAITV(16); }
          else if (c == 6) WAITV(8); else WAITV(0);
        }
        BAR();
      } else if (w >= 4) {
        const int hw = w - 4;
        f32x4 s1[2] = {{0, 0, 0, 0}, {0, 0, 0, 0}}, s2[2] = {{0, 0, 0, 0}, {0, 0, 0, 0}}, cs[2] = {{0, 0, 0, 0}, {0, 0, 0, 0}};
        const s16x8 ones = {0x3f80, 0x3f80, 0x3f80, 0x3f80, 0x3f80, 0x3f80, 0x3f80, 0x3f80};
        const u32 wadr = 16384 + (8 * g + q4) * 256 + ((hw ^ q4) * 64) + 8 * p4;
        const int f7 = (l15 >> 1) & 7;
        const u32 xadr0 = (32 * hw + l15) * 128 + ((g ^ f7) * 16), xadr1 = (32 * hw + l15) * 128 + (((4 + g) ^ f7) * 16);
        BAR();
        u32 base = L0;
#pragma unroll 1
        for (int c = 0; c < 10; ++c) {
          BAR();
#pragma unroll
          for (int ks = 0; ks < 2; ++ks)
#pragma unroll
            for (int j = 0; j < 2; ++j) {
              const s16x8 xf = lds_b128(base + (ks ? xadr1 : xadr0) + j * 2048);
              const u32 a = base + wadr + ks * 8192 + j * 32;
              s1[j] = mfma16(ones, xf, s1[j]); s2[j] = mfma16(xf, xf, s2[j]);
              cs[j] = mfma16(cat(lds_tr(a), lds_tr(a + 1024)), ones, cs[j]);
            }
          base = base == L0 + 4 * 32768 ? L0 : base + 32768;
        }
#pragma unroll
        for (int j = 0; j < 2; ++j) {
          const float sq = (l15 & 2) ? ((l15 & 1) ? s2[j][3] : s2[j][2]) : ((l15 & 1) ? s2[j][1] : s2[j][0]);
          const float mean = s1[j][0] * (1.0f / DM), var = fmaxf(sq * (1.0f / DM) - mean * mean, 0.f);
          float m, r; ln_stats(mean, var, m, r);
          if (g == q4) *(LAS f32x2*)(size_t)(L0 + TAB + (32 * hw + 16 * j + l15) * 8) = (f32x2){m, r};
          if (l15 == 0) *(LAS f32x4*)(size_t)(L0 + TAB + 1024 + ((2 * hw + j) * 16 + 4 * g) * 4) = cs[j];
        }
        BAR();
      } else {
        const int wr = w >> 1, wc = w & 1, r31 = lane & 31, h = lane >> 5, blk = g & 1;
        f32x16 acc[2][2];
#pragma unroll
        for (int i = 0; i < 2; ++i)
#pragma unroll
          for (int j = 0; j < 2; ++j)
#pragma unroll
            for (int r = 0; r < 16; ++r) acc[i][j][r] = 0.f;
        u32 xa[4], wa[2];
        {
          const int row = 64 * wr + r31, f = (row >> 1) & 7;
#pragma unroll
          for (int kk = 0; kk < 4; ++kk) xa[kk] = row * 128 + (((2 * kk + h) ^ f) * 16);
#pragma unroll
          for (int j = 0; j < 2; ++j) wa[j] = 16384 + (8 * h + q4) * 256 + (((2 * wc + j) ^ q4) * 64) + blk * 32 + 8 * p4;
        }
        s16x8 xA[2][2], wA[2][2], xB[2][2], wB[2][2];
#define P1_READ(X, W, base, ks) do { _Pragma("unroll") for (int kk = 0; kk < 2; ++kk) { \
          _Pragma("unroll") for (int j = 0; j < 2; ++j) { const u32 a_ = (base) + wa[j] + (2 * (ks) + kk) * 4096; W[kk][j] = cat(lds_tr(a_), lds_tr(a_ + 1024)); } \
          _Pragma("unroll") for (int i = 0; i < 2; ++i) X[kk][i] = lds_b128((base) + xa[2 * (ks) + kk] + i * 4096); } } while (0)
#define P1_MMA(X, W) do { _Pragma("unroll") for (int kk = 0; kk < 2; ++kk) _Pragma("unroll") for (int i = 0; i < 2; ++i) _Pragma("unroll") for (int j = 0; j < 2; ++j) \
          acc[i][j] = mfma32(W[kk][j], X[kk][i], acc[i][j]); } while (0)
#define P1_SCHED() do { _Pragma("unroll") for (int z = 0; z < 6; ++z) { SGB(M_MFMA, 1); SGB(M_DSR, 2); } SGB(M_MFMA, 2); SCHED0(); } while (0)
        BAR();
        CSTAMP(28);
        P1_READ(xA, wA, L0, 0);
        u32 base = L0;
#pragma unroll 1
        for (int c = 0; c < 10; ++c) {
          PBAR(); SCHED0();
          P1_READ(xB, wB, base, 1);
          P1_MMA(xA, wA);
          P1_SCHED();
          base = base == L0 + 4 * 32768 ? L0 : base + 32768;
          P1_READ(xA, wA, base, 0);
          P1_MMA(xB, wB);
          P1_SCHED();
        }
        STAMP(12);
        CSTAMP(29);
        BAR();
        STAMP(13);
        const float qs = ct < 5 ? 0.22360679775f * 1.44269504089f : 1.0f;
        const __amdgpu_buffer_rsrc_t rO = mk_rs(ws + WS_QKV, SZ_QKV);
#pragma unroll
        for (int i = 0; i < 2; ++i) {
          const int row = 64 * wr + 32 * i + r31;
          const f32x2 st = *(LAS f32x2*)(size_t)(L0 + TAB + row * 8);
          const float rr = st[1] * qs;
#pragma unroll
          for (int j = 0; j < 2; ++j) {
            u32 pk[4][2];
#pragma unroll
            for (int qd = 0; qd < 4; ++qd) {
              const f32x4 cj = *(LAS f32x4*)(size_t)(L0 + TAB + 1024 + (64 * wc + 32 * j + 8 * qd + 4 * h) * 4);
              const f32x4 a4 = {acc[i][j][4 * qd], acc[i][j][4 * qd + 1], acc[i][j][4 * qd + 2], acc[i][j][4 * qd + 3]};
              const f32x4 v = (a4 - cj * st[0]) * rr;
              pk[qd][0] = pk_bf16(v[0], v[1]); pk[qd][1] = pk_bf16(v[2], v[3]);
            }
#pragma unroll
            for (int o = 0; o < 4; o += 2) {
              auto s0 = __builtin_amdgcn_permlane32_swap(pk[o][0], pk[o + 1][0], false, false);
              auto s1 = __builtin_amdgcn_permlane32_swap(pk[o][1], pk[o + 1][1], false, false);
              __builtin_amdgcn_raw_buffer_store_b128((u32x4){s0[0], s1[0], s0[1], s1[1]}, rO, (u32)((16 * ct + 8 * wc + 4 * j + o + h) * 32768 + (128 * RT + row) * 16), 0, 16);
            }
          }
        }
        STAMP(14);
        WAITV(0);
        STAMP(15);
      }
      BAR();
      if (multi && tid == 0) sy.post(FL_QKV + RT * 15 + ct);
    } else {
      colsum_job(wg, wu, ws, 2 * (4 * (T >> 1) + s - 28), L0 + 96 * 1024, tid, lane, w);
      if (multi && tid == 0) sy.post(FL_CS + 4 * (T >> 1) + s - 28);
    }
  }
  if (w == 7) asm volatile("s_waitcnt vmcnt(0)" : "+v"(pfd));
  STAMP(1);

  if (ph_lo <= 2 && 2 < ph_hi) {
    const int QB = s >> 2, hd = 4 * T + (s & 3);
    constexpr u32 PL = 8256, LK = 81920, LV = LK + 3 * PL;
    const int off = hd & 1, P0 = (5 * hd) >> 1;
    if (tid < 512) *(LAS u32x4*)(size_t)(L0 + LV + 3 * PL + tid * 16) = (u32x4){0x00003f80u, 0u, 0x00003f80u, 0u};
    if (multi && w == 0) {
      int idx = -1;
      if (lane < 24) {
        const int sec = lane >> 3, r4 = (lane >> 1) & 3, e = lane & 1;
        const int RT = 2 * QB - 2 + r4;
        if (RT >= 0 && !(sec == 0 && r4 < 2)) idx = FL_QKV + RT * 15 + 5 * sec + (20 * hd + 19 * e) / 128;
      }
      sy.wait(idx);
    }
    BAR();
    STAMP(2);
    if (w >= 8) {
      const u32x4 rQ = mk_rsrc(ws + WS_QKV, SZ_QKV);
      if (QB > 0 || lw >= 2) {
#pragma unroll 1
        for (int i = 0; i < 6; ++i) {
          const int tv = i >= 3, j = i - 3 * tv;
          const u32 so = (u32)((80 + 80 * tv + P0 + j) * 32768 + (256 * (QB - 1) + 128 * lw) * 16), ld = L0 + (tv ? LV : LK) + j * PL + lw * 2048;
          dma_c(rQ, lane * 16, so, ld); dma_c(rQ, lane * 16, so + 1024, ld + 1024);
        }
      }
      WAITV(0);
      BAR();
      if (multi && own35) prefetch_wo();
    } else {
      const int q = lane & 31, h = lane >> 5;
      s16x8 q1; s16x4 q2;
      {
        const __amdgpu_buffer_rsrc_t rQ = mk_rs(ws + WS_QKV, SZ_QKV);
        const u32 tok = (u32)(256 * QB + 32 * w + q) * 16;
        q1 = __builtin_bit_cast(s16x8, __builtin_amdgcn_raw_buffer_load_b128(rQ, (P0 + off + h) * 32768 + tok, 0, 16));
        q2 = __builtin_bit_cast(s16x4, __builtin_amdgcn_raw_buffer_load_b64(rQ, h ? OOB : (P0 + 2 - 2 * off) * 32768 + tok + 8 * off, 0, 16));
      }
      BAR();
      STAMP(16);
      const u32 kadr = L0 + LK + (off + h) * PL + (32 * w + q) * 16;
      const u32 k2adr = L0 + LK + (2 - 2 * off) * PL + (32 * w + q) * 16 + 8 * off;
      const int jq = 4 * (g & 1) + p4;
      const int lq = jq < 5 ? jq + off : (jq < 7 ? jq + 1 : (off ? 0 : 5));
      const u32 vadr = L0 + LV + (lq >> 1) * PL + (32 * w + 4 * h + q4) * 16 + (lq & 1) * 8;
      const f32x16 zero16 = {0, 0, 0, 0, 0, 0, 0, 0, 0, 0, 0, 0, 0, 0, 0, 0};
      const int qd = q - 4 * h;
      constexpr float NEG = -3.0e38f;
      auto score = [&](int j, const f32x16& c) -> f32x16 {
        return mfma32k8(lds_b64(k2adr + j * 512), q2, mfma32(lds_b128(kadr + j * 512), q1, c));
      };
      auto pv = [&](int j, int ks, u32 p0, u32 p1, u32 p2, u32 p3, const f32x16& o) -> f32x16 {
        const u32 a = vadr + j * 512 + ks * 256;
        return mfma32(cat(lds_tr(a), lds_tr(a + 128)), __builtin_bit_cast(s16x8, (u32x4){p0, p1, p2, p3}), o);
      };
      f32x16 o = zero16;
      float m = NEG;
      const int jstart = QB > 0 ? 1 : (8 - w > 1 ? 8 - w : 1);
      if (QB > 0) {
        const f32x16 sf = score(0, zero16), sd = score(8, zero16);
#pragma unroll
        for (int r = 0; r < 16; r += 2) {
          const float e0 = ((r & 3) + 8 * (r >> 2) <= qd) ? sd[r] : sf[r], e1 = (((r + 1) & 3) + 8 * ((r + 1) >> 2) <= qd) ? sd[r + 1] : sf[r + 1];
          m = fmaxf(fmaxf(m, e0), e1);
        }
#pragma unroll
        for (int j = 1; j < 8; ++j) {
          const f32x16 sc = score(j, zero16);
#pragma unroll
          for (int r = 0; r < 16; r += 2) m = fmaxf(fmaxf(m, sc[r]), sc[r + 1]);
        }
      } else {
        const f32x16 sd = score(8, zero16);
#pragma unroll
        for (int r = 0; r < 16; ++r) m = fmaxf(m, ((r & 3) + 8 * (r >> 2) <= qd) ? sd[r] : NEG);
#pragma unroll 1
        for (int j = jstart; j < 8; ++j) {
          const f32x16 sc = score(j, zero16);
#pragma unroll
          for (int r = 0; r < 16; r += 2) m = fmaxf(fmaxf(m, sc[r]), sc[r + 1]);
        }
      }
      m = xhalf_max(m);
      STAMP(17);
      asm volatile("" ::: "memory");
      f32x16 negm;
#pragma unroll
      for (int r = 0; r < 16; ++r) negm[r] = -m;
      auto full = [&](int j) {
        const f32x16 sc = score(j, negm);
        u32 pk[8];
#pragma unroll
        for (int i = 0; i < 8; ++i) pk[i] = pk_bf16(__builtin_amdgcn_exp2f(sc[2 * i]), __builtin_amdgcn_exp2f(sc[2 * i + 1]));
        o = pv(j, 0, pk[0], pk[1], pk[2], pk[3], o);
        o = pv(j, 1, pk[4], pk[5], pk[6], pk[7], o);
      };
      if (QB > 0) {
#pragma unroll
        for (int j = 1; j < 8; ++j) full(j);
      } else {
#pragma unroll 1
        for (int j = jstart; j < 8; ++j) full(j);
      }
      {
        const f32x16 sd = score(8, negm);
        f32x16 sf;
        if (QB > 0) sf = score(0, negm);
        else {
#pragma unroll
          for (int r = 0; r < 16; ++r) sf[r] = NEG;
        }
        u32 pd[8], pf[8];
#pragma unroll
        for (int i = 0; i < 8; ++i) {
          const bool v0 = ((2 * i) & 3) + 8 * ((2 * i) >> 2) <= qd, v1 = ((2 * i + 1) & 3) + 8 * ((2 * i + 1) >> 2) <= qd;
          const u32 pk = pk_bf16(__builtin_amdgcn_exp2f(v0 ? sd[2 * i] : sf[2 * i]), __builtin_amdgcn_exp2f(v1 ? sd[2 * i + 1] : sf[2 * i + 1]));
          const u32 mk = (v0 ? 0xffffu : 0u) | (v1 ? 0xffff0000u : 0u);
          pd[i] = pk & mk; pf[i] = pk & ~mk;
        }
        o = pv(8, 0, pd[0], pd[1], pd[2], pd[3], o);
        o = pv(8, 1, pd[4], pd[5], pd[6], pd[7], o);
        if (QB > 0) {
          o = pv(0, 0, pf[0], pf[1], pf[2], pf[3], o);
          o = pv(0, 1, pf[4], pf[5], pf[6], pf[7], o);
        }
      }
      STAMP(18);
      auto r = __builtin_amdgcn_permlane32_swap(__float_as_uint(o[8]), __float_as_uint(o[8]), false, false);
      const float inv = 1.0f / __uint_as_float(r[1]);
      const __amdgpu_buffer_rsrc_t rO = mk_rs(ws + WS_O, SZ_O);
      const u32 tok = (u32)(256 * QB + 32 * w + q) * 8;
#pragma unroll
      for (int i = 0; i < 3; ++i) {
        const u32x2 v = {pk_bf16(o[4 * i] * inv, o[4 * i + 1] * inv), pk_bf16(o[4 * i + 2] * inv, o[4 * i + 3] * inv)};
        __builtin_amdgcn_raw_buffer_store_b64(v, rO, (i == 2 && h) ? OOB : (u32)((5 * hd + 2 * i + h) * 16384) + tok, 0, 16);
      }
      WAITV(0);
    }
    BAR();
    if (multi && tid == 0) sy.post(FL_ATT + QB * 32 + hd);
  }
  STAMP(3);

  u32 hres[3][2][2];
  const int wr3 = (w >> 1) & 1, wc3 = w & 1, kh = (w >> 2) & 1;
  u32 nb_adr[2];
  {
    const int sw = ((q4 >> 1) & 1) | ((g & 1) << 1);
#pragma unroll
    for (int j = 0; j < 2; ++j) nb_adr[j] = (8 * g + q4) * 128 + (((2 * wc3 + j) ^ sw) * 32) + 8 * p4;
  }
#define N_MMA(X, W) do { _Pragma("unroll") for (int ks = 0; ks < 2; ++ks) _Pragma("unroll") for (int i = 0; i < 3; ++i) _Pragma("unroll") for (int j = 0; j < 2; ++j) \
      acc[i][j] = mfma16(W[ks][j], X[ks][i], acc[i][j]); } while (0)
#define N_REDUCE(base) do { \
    if (kh) { _Pragma("unroll") for (int i = 0; i < 3; ++i) _Pragma("unroll") for (int j = 0; j < 2; ++j) *(LAS f32x4*)(size_t)((base) + ((w & 3) * 6 + i * 2 + j) * 1024 + lane * 16) = acc[i][j]; } \
    BAR(); \
    if (!kh) { _Pragma("unroll") for (int i = 0; i < 3; ++i) _Pragma("unroll") for (int j = 0; j < 2; ++j) acc[i][j] += *(LAS f32x4*)(size_t)((base) + ((w & 3) * 6 + i * 2 + j) * 1024 + lane * 16); } } while (0)

  if (ph_lo <= 3 && 3 < ph_hi) {
    if (own35) {
      constexpr u32 LA = 81920;
      u32x2 xr[3][2];
      if (w < 4) {
        const __amdgpu_buffer_rsrc_t rX = mk_rs(x, SEQ * DM * 2);
#pragma unroll
        for (int i = 0; i < 3; ++i)
#pragma unroll
          for (int j = 0; j < 2; ++j) {
            const int r = 48 * wr3 + 16 * i + l15;
            xr[i][j] = __builtin_amdgcn_raw_buffer_load_b64(rX, r < nrow3 ? (u32)((row3 + r) * 1280 + (64 * ct3 + 32 * wc3 + 16 * j + 4 * g) * 2) : OOB, 0, 0);
          }
      }
      if (!multi && w >= 8) prefetch_wo();
      if (multi && w == 0) sy.wait(lane < 32 ? FL_ATT + T * 32 + lane : -1);
      BAR();
      STAMP(4);
      if (w >= 8) {
        const u32x4 rO = mk_rsrc(ws + WS_O, SZ_O);
        u32 vA[3];
#pragma unroll
        for (int ii = 0; ii < 3; ++ii) {
          const int u = (lw * 3 + ii) * 64 + lane, pl = u / 48, un = (u - pl * 48) ^ (((pl >> 1) & 1) << 3);
          vA[ii] = 2 * un < nrow3 ? (u32)(pl * 16384 + (row3 + 2 * un) * 8) : OOB;
        }
        auto issue = [&](int c, int slot) {
#pragma unroll
          for (int ii = 0; ii < 3; ++ii) dma_c(rO, vA[ii], c * (16 * 16384), L0 + LA + slot * 12288 + (lw * 3 + ii) * 1024);
        };
#pragma unroll 1
        for (int c = 0; c < 4; ++c) issue(c, c);
        WAITV(6); BAR();
        issue(4, 4); issue(5, 5);
        WAITV(6);
#pragma unroll 1
        for (int i = 0; i < 5; ++i) {
          BAR();
          if (i < 2) { issue(2 * i + 6, 2 * i); issue(2 * i + 7, 2 * i + 1); WAITV(6); } else WAITV(0);
        }
        BAR();
      } else {
        f32x4 acc[3][2];
#pragma unroll
        for (int i = 0; i < 3; ++i) { acc[i][0] = (f32x4){0, 0, 0, 0}; acc[i][1] = (f32x4){0, 0, 0, 0}; }
        u32 aadr[3];
#pragma unroll
        for (int i = 0; i < 3; ++i) {
          const int tok = 48 * wr3 + 16 * i + l15;
          aadr[i] = LA + kh * 12288 + (2 * g) * 768 + (((tok >> 1) ^ ((g & 1) << 3)) * 16) + (tok & 1) * 8;
        }
        s16x8 xA[2][3], wA[2][2], xB[2][3], wB[2][2];
#define P3_READ(X, W, sa, sb) do { _Pragma("unroll") for (int ks = 0; ks < 2; ++ks) { \
          _Pragma("unroll") for (int j = 0; j < 2; ++j) { const u32 a_ = (sb) + nb_adr[j] + ks * 4096; W[ks][j] = cat(lds_tr(a_), lds_tr(a_ + 512)); } \
          _Pragma("unroll") for (int i = 0; i < 3; ++i) { const u32 a_ = (sa) + aadr[i] + ks * 8 * 768; X[ks][i] = cat(lds_b64(a_), lds_b64(a_ + 768)); } } } while (0)
#define P3_SCHED() do { _Pragma("unroll") for (int z = 0; z < 10; ++z) { SGB(M_MFMA, 1); SGB(M_DSR, 2); } SGB(M_MFMA, 2); SCHED0(); } while (0)
        const u32 wb = L0 + kh * 8192;
        BAR();
        P3_READ(xA, wA, L0, wb);
#pragma unroll 1
        for (int st = 0; st < 4; st += 2) {
          BAR(); SCHED0();
          P3_READ(xB, wB, L0 + (st == 0 ? 1 : 0) * 24576, wb + (st + 1) * 16384);
          N_MMA(xA, wA);
          P3_SCHED();
          BAR(); SCHED0();
          P3_READ(xA, wA, L0 + (st == 0 ? 2 : 1) * 24576, wb + (st + 2) * 16384);
          N_MMA(xB, wB);
          P3_SCHED();
        }
        BAR();
        N_MMA(xA, wA);
        N_REDUCE(L0);
        STAMP(19);
        if (w < 4) {
          const __amdgpu_buffer_rsrc_t rH = mk_rs(ws + WS_H, SZ_H), rS = mk_rs(ws + WS_HST, SZ_HST);
#pragma unroll
          for (int i = 0; i < 3; ++i) {
            float hv[8];
#pragma unroll
            for (int j = 0; j < 2; ++j) {
              const u32 a0 = pk_bf16(acc[i][j][0], acc[i][j][1]), a1 = pk_bf16(acc[i][j][2], acc[i][j][3]);
              hres[i][j][0] = pk_bf16(bf_lo(xr[i][j][0]) + bf_lo(a0), bf_hi(xr[i][j][0]) + bf_hi(a0));
              hres[i][j][1] = pk_bf16(bf_lo(xr[i][j][1]) + bf_lo(a1), bf_hi(xr[i][j][1]) + bf_hi(a1));
              hv[4 * j] = bf_lo(hres[i][j][0]); hv[4 * j + 1] = bf_hi(hres[i][j][0]); hv[4 * j + 2] = bf_lo(hres[i][j][1]); hv[4 * j + 3] = bf_hi(hres[i][j][1]);
            }
            auto s0 = __builtin_amdgcn_permlane16_swap(hres[i][0][0], hres[i][1][0], false, false);
            auto s1 = __builtin_amdgcn_permlane16_swap(hres[i][0][1], hres[i][1][1], false, false);
            const int r = 48 * wr3 + 16 * i + l15;
            const int oct = 8 * ct3 + 4 * wc3 + ((g & 1) << 1) + (g >> 1);
            __builtin_amdgcn_raw_buffer_store_b128((u32x4){s0[0], s1[0], s0[1], s1[1]}, rH, r < nrow3 ? (u32)(oct * 32768 + (row3 + r) * 16) : OOB, 0, 16);
            float sm = 0.f;
#pragma unroll
            for (int e = 0; e < 8; ++e) sm += hv[e];
            const float mw = sum_x16_x32(sm) * (1.0f / 32.0f);
            float m2 = 0.f;
#pragma unroll
            for (int e = 0; e < 8; ++e) { const float d = hv[e] - mw; m2 += d * d; }
            m2 = sum_x16_x32(m2);
            __builtin_amdgcn_raw_buffer_store_b64((u32x2){__float_as_uint(mw), __float_as_uint(m2)}, rS,
                                                  (g == 0 && r < nrow3) ? (u32)(((T * 20 + 2 * ct3 + wc3) * 256 + 96 * rt3 + r) * 8) : OOB, 0, 16);
          }
          WAITV(0);
        }
      }
      BAR();
      if (multi && tid == 0) sy.post(FL_H + T * 30 + s);
    } else {
      const int job = 16 + 2 * T + s - 30;
      if (job < 27) {
        colsum_job(wg, wu, ws, 2 * job, L0 + 96 * 1024, tid, lane, w);
        if (multi && tid == 0) sy.post(FL_CS + job);
      }
    }
  }
  STAMP(5);

  if (ph_lo <= 4 && 4 < ph_hi) {
    const int R4 = 4 * (T & 1) + (s & 3), c4 = 7 * (T >> 1) + (s >> 2);
    if (s < 28 && c4 < 27) {
      constexpr u32 ST = 24576;
      if (w >= 8) {
        const u32x4 rA = mk_rsrc(ws + WS_H, SZ_H), rG = mk_rsrc(wg, DM * FF * 2), rU = mk_rsrc(wu, DM * FF * 2);
        const u32 vN = [&] { const int k = 8 * lw + (lane >> 3); return (u32)(k * (FF * 2) + c4 * 128 + ((((lane & 7) >> 2) ^ ((k >> 1) & 1)) * 64) + (lane & 3) * 16); }();
        auto issueB = [&](int t, int slot) {
          dma(rG, vN, t * (32 * FF * 2), L0 + slot * ST + 16384 + lw * 1024);
          dma(rU, vN, t * (32 * FF * 2), L0 + slot * ST + 20480 + lw * 1024);
        };
        const u32 vA = (u32)((256 * R4 + lane) * 16);
        auto issueA = [&](int t, int slot) {
#pragma unroll
          for (int ii = 0; ii < 4; ++ii) dma_c(rA, vA, (4 * t + lw) * 32768 + ii * 1024, L0 + slot * ST + lw * 4096 + ii * 1024);
        };
#pragma unroll 1
        for (int t = 0; t < 5; ++t) issueB(t, t);
        BAR();
        issueA(0, 0); issueA(1, 1);
        WAITV(4); BAR();
        issueA(2, 2);
        WAITV(4);
        int slot = 5;
#pragma unroll 1
        for (int t = 0; t < 15; ++t) {
          BAR();
          if (t == 0) { issueA(3, 3); issueA(4, 4); }
          issueA(t + 5, slot); issueB(t + 5, slot); slot = slot == 5 ? 0 : slot + 1;
          if (t < 2) WAITV(14); else WAITV(18);
        }
        asm volatile("" ::: "memory");
        const int row = tid - 512;
        const __amdgpu_buffer_rsrc_t rS = mk_rs(ws + WS_HST, SZ_HST), rC = mk_rs(ws + WS_CS, SZ_CS);
        u32x2 pp[20];
#pragma unroll
        for (int i = 0; i < 20; ++i) pp[i] = __builtin_amdgcn_raw_buffer_load_b64(rS, (u32)(((R4 * 20 + i) * 256 + row) * 8), 0, 16);
        const u32 csv = __builtin_amdgcn_raw_buffer_load_b32(rC, (u32)((((row >> 6) & 1) * 27 + c4) * 256 + (row & 63) * 4), 0, 16);
        BAR(); WAITV(33);
        BAR(); WAITV(27);
        BAR(); WAITV(0);
        {
          float sm = 0.f;
#pragma unroll
          for (int i = 0; i < 20; ++i) sm += __uint_as_float(pp[i][0]);
          const float mean = sm * 0.05f;
          float m2 = 0.f;
#pragma unroll
          for (int i = 0; i < 20; ++i) { const float d = __uint_as_float(pp[i][0]) - mean; m2 += __uint_as_float(pp[i][1]) + 32.0f * d * d; }
          float m, r; ln_stats(mean, m2 * (1.0f / DM), m, r);
          *(LAS f32x2*)(size_t)(L0 + L_TAB + row * 8) = (f32x2){m, r};
          if (row < 128) *(LAS u32*)(size_t)(L0 + L_TAB + 2048 + row * 4) = csv;
        }
        BAR(); BAR();
        BAR();
      } else {
        if (multi && w == 0) {
          int idx = -1;
          if (lane < 30) idx = FL_H + R4 * 30 + lane;
          else if (lane == 30) idx = FL_CS + (c4 >> 1);
          else if (lane == 31) idx = FL_CS + ((27 + c4) >> 1);
          sy.wait(idx);
        }
        BAR();
        STAMP(6);
        const int wr = w >> 1, wc = w & 1, r31 = lane & 31, h = lane >> 5, blk = g & 1;
        f32x16 ag[2], au[2];
#pragma unroll
        for (int i = 0; i < 2; ++i)
#pragma unroll
          for (int r = 0; r < 16; ++r) { ag[i][r] = 0.f; au[i][r] = 0.f; }
        const u32 xadr = h * 4096 + (64 * wr + r31) * 16;
        const u32 wadr = 16384 + (8 * h + q4) * 128 + ((wc ^ ((q4 >> 1) & 1)) * 64) + blk * 32 + 8 * p4;
        s16x8 xA[2][2], gA[2], uA[2], xB[2][2], gB[2], uB[2];
#define P4_READ(X, G_, U_, base) do { _Pragma("unroll") for (int kk = 0; kk < 2; ++kk) { const u32 a_ = (base) + wadr + kk * 2048; \
          G_[kk] = cat(lds_tr(a_), lds_tr(a_ + 512)); U_[kk] = cat(lds_tr(a_ + 4096), lds_tr(a_ + 4096 + 512)); \
          _Pragma("unroll") for (int i = 0; i < 2; ++i) X[kk][i] = lds_b128((base) + xadr + kk * 8192 + i * 512); } } while (0)
#define P4_MMA(X, G_, U_) do { _Pragma("unroll") for (int kk = 0; kk < 2; ++kk) _Pragma("unroll") for (int i = 0; i < 2; ++i) { \
          ag[i] = mfma32(G_[kk], X[kk][i], ag[i]); au[i] = mfma32(U_[kk], X[kk][i], au[i]); } } while (0)
#define P4_SCHED() do { _Pragma("unroll") for (int z = 0; z < 6; ++z) { SGB(M_MFMA, 1); SGB(M_DSR, 2); } SGB(M_MFMA, 2); SCHED0(); } while (0)
        BAR();
        STAMP(20);
        CSTAMP(26);
        P4_READ(xA, gA, uA, L0);
        u32 base = L0 + ST;
#pragma unroll 1
        for (int t = 0; t < 20; t += 2) {
          PBAR(); SCHED0();
          P4_READ(xB, gB, uB, base);
          P4_MMA(xA, gA, uA);
          P4_SCHED();
          base = base == L0 + 5 * ST ? L0 : base + ST;
          PBAR(); SCHED0();
          P4_READ(xA, gA, uA, base);
          P4_MMA(xB, gB, uB);
          P4_SCHED();
          base = base == L0 + 5 * ST ? L0 : base + ST;
        }
        BAR();
        STAMP(21);
        CSTAMP(27);
        const __amdgpu_buffer_rsrc_t rH = mk_rs(ws + WS_G, SZ_G);
#pragma unroll
        for (int i = 0; i < 2; ++i) {
          const int row = 64 * wr + 32 * i + r31;
          const f32x2 st = *(LAS f32x2*)(size_t)(L0 + L_TAB + row * 8);
          const float r2 = st[1] * st[1];
          u32 pk[4][2];
#pragma unroll
          for (int qd = 0; qd < 4; ++qd) {
            const int n = 32 * wc + 8 * qd + 4 * h;
            const f32x4 cg = *(LAS f32x4*)(size_t)(L0 + L_TAB + 2048 + n * 4), cu = *(LAS f32x4*)(size_t)(L0 + L_TAB + 2048 + 256 + n * 4);
            const f32x4 g4 = {ag[i][4 * qd], ag[i][4 * qd + 1], ag[i][4 * qd + 2], ag[i][4 * qd + 3]}, u4 = {au[i][4 * qd], au[i][4 * qd + 1], au[i][4 * qd + 2], au[i][4 * qd + 3]};
            f32x4 a = g4 - cg * st[0];
            const f32x4 u = (u4 - cu * st[0]) * r2;
#pragma unroll
            for (int e = 0; e < 4; ++e) a[e] = fmaxf(a[e], 0.f) * u[e];
            pk[qd][0] = pk_bf16(a[0], a[1]); pk[qd][1] = pk_bf16(a[2], a[3]);
          }
#pragma unroll
          for (int o = 0; o < 4; o += 2) {
            auto s0 = __builtin_amdgcn_permlane32_swap(pk[o][0], pk[o + 1][0], false, false);
            auto s1 = __builtin_amdgcn_permlane32_swap(pk[o][1], pk[o + 1][1], false, false);
            __builtin_amdgcn_raw_buffer_store_b128((u32x4){s0[0], s1[0], s0[1], s1[1]}, rH, (u32)((8 * c4 + 4 * wc + o + h) * 32768 + (256 * R4 + row) * 16), 0, 16);
          }
        }
        STAMP(22);
        WAITV(0);
      }
      BAR();
      if (multi && tid == 0) sy.post(FL_FF + R4 * 27 + c4);
    } else if (multi && s >= 28 && w >= 8) {
      const u32x4 rW = mk_rsrc(wd, FF * DM * 2);
#pragma unroll 1
      for (int i = 0; i < 135; ++i) {
        dma(rW, lane * 16, (u32)(((s - 28) * 540 + lw * 135 + i) * 1024), L0 + (lw * 16 + (i & 15)) * 1024);
        WAITV(24);
      }
      WAITV(0);
    }
  }
  STAMP(7);

  if (ph_lo <= 5 && 5 < ph_hi) {
    if (own35) {
      constexpr u32 LA = 81920;
      if (w >= 8) {
        const u32x4 rW = mk_rsrc(wd, FF * DM * 2), rG = mk_rsrc(ws + WS_G, SZ_G);
        const u32 vB0 = bn_voff(2 * lw, 1280, ct3 * 128), vB1 = bn_voff(2 * lw + 1, 1280, ct3 * 128);
        auto issueB = [&](int i, int slot) {
#pragma unroll
          for (int cc = 0; cc < 2; ++cc) {
            const int c = 2 * i + cc > 26 ? 26 : 2 * i + cc;
            dma(rW, vB0, c * (64 * 1280), L0 + slot * 16384 + cc * 8192 + lw * 2048);
            dma(rW, vB1, c * (64 * 1280), L0 + slot * 16384 + cc * 8192 + lw * 2048 + 1024);
          }
        };
#pragma unroll 1
        for (int i = 0; i < 5; ++i) issueB(i, i);
        u32 vA[3];
#pragma unroll
        for (int ii = 0; ii < 3; ++ii) {
          const int u = (lw * 3 + ii) * 64 + lane, pl = u / 96, tk = u - pl * 96;
          vA[ii] = tk < nrow3 ? (u32)(pl * 32768 + (row3 + tk) * 16) : OOB;
        }
        auto issueA = [&](int i, int slot) {
#pragma unroll
          for (int cc = 0; cc < 2; ++cc) {
            const int c = 2 * i + cc > 26 ? 26 : 2 * i + cc;
#pragma unroll
            for (int ii = 0; ii < 3; ++ii) dma_c(rG, vA[ii], c * (8 * 32768), L0 + LA + slot * 24576 + cc * 12288 + (lw * 3 + ii) * 1024);
          }
        };
        BAR();
        issueA(0, 0); issueA(1, 1);
        WAITV(6); BAR();
        issueA(2, 2);
        WAITV(6);
        int sa = 0, sb = 0;
#pragma unroll 1
        for (int i = 0; i < 14; ++i) {
          BAR();
          if (i <= 10) { issueA(i + 3, sa); sa = sa == 2 ? 0 : sa + 1; }
          if (i <= 8) { issueB(i + 5, sb); sb = sb == 4 ? 0 : sb + 1; }
          if (i >= 1 && i <= 8) WAITV(14); else if (i == 0 || i == 9) WAITV(10); else if (i == 10) WAITV(6); else WAITV(0);
        }
        BAR();
      } else {
        if (!multi && w < 4) {
          const __amdgpu_buffer_rsrc_t rH = mk_rs(ws + WS_H, SZ_H);
#pragma unroll
          for (int i = 0; i < 3; ++i)
#pragma unroll
            for (int j = 0; j < 2; ++j) {
              const int r = 48 * wr3 + 16 * i + l15, col = 64 * ct3 + 32 * wc3 + 16 * j + 4 * g;
              const u32x2 v = __builtin_amdgcn_raw_buffer_load_b64(rH, r < nrow3 ? (u32)((col >> 3) * 32768 + (row3 + r) * 16 + (col & 7) * 2) : OOB, 0, 0);
              hres[i][j][0] = v[0]; hres[i][j][1] = v[1];
            }
        }
        if (multi && w == 0) sy.wait(lane < 27 ? FL_FF + T * 27 + lane : -1);
        BAR();
        STAMP(8);
        f32x4 acc[3][2];
#pragma unroll
        for (int i = 0; i < 3; ++i) { acc[i][0] = (f32x4){0, 0, 0, 0}; acc[i][1] = (f32x4){0, 0, 0, 0}; }
        const u32 aadr = LA + kh * 12288 + g * 1536 + (48 * wr3 + l15) * 16;
        s16x8 xA[2][3], wA[2][2], xB[2][3], wB[2][2];
#define P5_READ(X, W, sa, sb) do { _Pragma("unroll") for (int ks = 0; ks < 2; ++ks) { \
          _Pragma("unroll") for (int j = 0; j < 2; ++j) { const u32 a_ = (sb) + nb_adr[j] + ks * 4096; W[ks][j] = cat(lds_tr(a_), lds_tr(a_ + 512)); } \
          _Pragma("unroll") for (int i = 0; i < 3; ++i) X[ks][i] = lds_b128((sa) + aadr + ks * 4 * 1536 + i * 256); } } while (0)
#define P5_SCHED() do { _Pragma("unroll") for (int z = 0; z < 12; ++z) { SGB(M_MFMA, 1); SGB(M_DSR, 1); } SGB(M_DSR, 2); SCHED0(); } while (0)
        BAR();
        P5_READ(xA, wA, L0, L0 + kh * 8192);
        u32 pa = L0 + 24576, pb = L0 + kh * 8192 + 16384;
        const u32 pa_end = L0 + 2 * 24576, pb_end = L0 + kh * 8192 + 4 * 16384;
#pragma unroll 1
        for (int i = 0; i < 12; i += 2) {
          BAR(); SCHED0();
          P5_READ(xB, wB, pa, pb);
          N_MMA(xA, wA);
          P5_SCHED();
          pa = pa == pa_end ? L0 : pa + 24576; pb = pb == pb_end ? L0 + kh * 8192 : pb + 16384;
          BAR(); SCHED0();
          P5_READ(xA, wA, pa, pb);
          N_MMA(xB, wB);
          P5_SCHED();
          pa = pa == pa_end ? L0 : pa + 24576; pb = pb == pb_end ? L0 + kh * 8192 : pb + 16384;
        }
        BAR(); SCHED0();
        P5_READ(xB, wB, pa, pb);
        N_MMA(xA, wA);
        P5_SCHED();
        BAR();
        if (!kh) N_MMA(xB, wB);
        N_REDUCE(L0);
        STAMP(23);
        if (w < 4) {
#pragma unroll
          for (int i = 0; i < 3; ++i) {
            u32 pk[2][2];
#pragma unroll
            for (int j = 0; j < 2; ++j) {
              const u32 a0 = pk_bf16(acc[i][j][0], acc[i][j][1]), a1 = pk_bf16(acc[i][j][2], acc[i][j][3]);
              pk[j][0] = pk_bf16(bf_lo(hres[i][j][0]) + bf_lo(a0), bf_hi(hres[i][j][0]) + bf_hi(a0));
              pk[j][1] = pk_bf16(bf_lo(hres[i][j][1]) + bf_lo(a1), bf_hi(hres[i][j][1]) + bf_hi(a1));
            }
            auto s0 = __builtin_amdgcn_permlane16_swap(pk[0][0], pk[1][0], false, false);
            auto s1 = __builtin_amdgcn_permlane16_swap(pk[0][1], pk[1][1], false, false);
            const int r = 48 * wr3 + 16 * i + l15;
            const int oct = 8 * ct3 + 4 * wc3 + ((g & 1) << 1) + (g >> 1);
            if (r < nrow3) *(u32x4*)(out + (size_t)(row3 + r) * DM + oct * 8) = (u32x4){s0[0], s1[0], s0[1], s1[1]};
          }
        }
      }
    }
    if (multi && b == 0 && w == 0) {
#pragma unroll 1
      for (int i = 0; i < 4; ++i) sy.wait(FL_ATT + i * 64 + lane);
      if (lane == 0) __hip_atomic_store((GAS u64*)(ws + WS_EPOCH), sy.tag, __ATOMIC_RELAXED, __HIP_MEMORY_SCOPE_AGENT);
    }
  }
  STAMP(9);
}

extern "C" void kernel_launch(void* const* d_in, const int* in_sizes, int n_in, void* d_out, int out_size, void* d_ws, size_t ws_size, hipStream_t stream) {
  (void)in_sizes; (void)n_in; (void)out_size; (void)ws_size;
  const u16* x = (const u16*)d_in[0]; const u16* wqkv = (const u16*)d_in[1]; const u16* wo = (const u16*)d_in[2];
  const u16* wg = (const u16*)d_in[4]; const u16* wu = (const u16*)d_in[5]; const u16* wd = (const u16*)d_in[6];
  (void)hipFuncSetAttribute((const void*)fused_block, hipFuncAttributeMaxDynamicSharedMemorySize, LDS_BYTES);
  fused_block<<<256, NTHR, LDS_BYTES, stream>>>(x, wqkv, wo, wg, wu, wd, (u16*)d_out, (char*)d_ws, 1, 6);
}
```

```cpp
#include <hip/hip_runtime.h>
#include <stdint.h>

typedef unsigned short u16;
typedef unsigned int u32;
typedef unsigned long long u64;
typedef short s16x4 __attribute__((ext_vector_type(4)));
typedef short s16x8 __attribute__((ext_vector_type(8)));
typedef __bf16 bf16x8 __attribute__((ext_vector_type(8)));
typedef __bf16 bf16x2 __attribute__((ext_vector_type(2)));
typedef float f32x2 __attribute__((ext_vector_type(2)));
typedef float f32x4 __attribute__((ext_vector_type(4)));
typedef float f32x16 __attribute__((ext_vector_type(16)));
typedef u32 u32x2 __attribute__((ext_vector_type(2)));
typedef u32 u32x4 __attribute__((ext_vector_type(4)));
#define LAS __attribute__((address_space(3)))
#define GAS __attribute__((address_space(1)))
#define DEV __device__ __forceinline__


constexpr int SEQ = 2048, DM = 640, FF = 1728;
constexpr u32 WS_EPOCH = 0;
constexpr u32 WS_FLAGS = 4096;
constexpr u32 FL_QKV = 0, FL_ATT = 256, FL_H = 512, FL_FF = 768, FL_CS = 1000, FL_SJ = 1100;
constexpr u32 WS_QKV = 512 * 1024;
constexpr u32 SZ_QKV = 240 * 32768;
constexpr u32 WS_O = WS_QKV + SZ_QKV;
constexpr u32 SZ_O = 160 * 16384;
constexpr u32 WS_H = WS_O + SZ_O;
constexpr u32 SZ_H = 80 * 32768;
constexpr u32 WS_HST = WS_H + SZ_H;
constexpr u32 SZ_HST = 8 * 20 * 256 * 8;
constexpr u32 WS_G = WS_HST + SZ_HST;
constexpr u32 SZ_G = 216 * 32768;
constexpr u32 WS_CS = WS_G + SZ_G;
constexpr u32 SZ_CS = 54 * 64 * 4;
constexpr u32 WS_XST = WS_CS + 16384;
constexpr u32 SZ_XST = 1024 * 128;
constexpr u32 WS_QCS = WS_XST + SZ_XST;
constexpr u32 SZ_QCS = 30 * 32 * 64 * 4;
constexpr u32 LDS_BYTES = 160 * 1024;
constexpr u32 L_TAB = 156 * 1024;
constexpr int NTHR = 768;
constexpr u32 OOB = 0x80000000u;

constexpr int CODE_KB = 36;

DEV u32x4 mk_rsrc(const void* p, u32 bytes) {
  const u64 a = (u64)p;
  u32x4 r; r[0] = (u32)a; r[1] = (u32)(a >> 32) & 0xffffu; r[2] = bytes; r[3] = 0x00020000u; return r;
}
DEV __amdgpu_buffer_rsrc_t mk_rs(const void* p, u32 bytes) { return __builtin_amdgcn_make_buffer_rsrc((void*)p, 0, bytes, 0x00020000); }
DEV void dma(u32x4 rs, u32 voff, u32 soff, u32 lds) {
  asm volatile("s_mov_b32 m0, %0\n\ts_nop 0\n\tbuffer_load_dwordx4 %1, %2, %3 offen lds" ::"s"(lds), "v"(voff), "s"(rs), "s"(soff));
}
DEV void dma_c(u32x4 rs, u32 voff, u32 soff, u32 lds) {
  asm volatile("s_mov_b32 m0, %0\n\ts_nop 0\n\tbuffer_load_dwordx4 %1, %2, %3 offen sc1 lds" ::"s"(lds), "v"(voff), "s"(rs), "s"(soff));
}
#define WAITV(n) asm volatile("s_waitcnt vmcnt(" #n ")" ::: "memory")
#define BAR() asm volatile("s_waitcnt lgkmcnt(0)\n\ts_barrier" ::: "memory")
DEV u32 pk_bf16(float lo, float hi) { f32x2 v = {lo, hi}; return __builtin_bit_cast(u32, __builtin_convertvector(v, bf16x2)); }
DEV float bf_lo(u32 d) { return __uint_as_float(d << 16); }
DEV float bf_hi(u32 d) { return __uint_as_float(d & 0xffff0000u); }
DEV float rbf(float x) { return bf_lo(pk_bf16(x, x)); }
DEV s16x8 lds_b128(u32 a) { return *(LAS s16x8*)(size_t)a; }
DEV s16x4 lds_b64(u32 a) { return *(LAS s16x4*)(size_t)a; }
DEV s16x4 lds_tr(u32 a) { return __builtin_amdgcn_ds_read_tr16_b64_v4i16((LAS s16x4*)(size_t)a); }
DEV s16x8 cat(s16x4 a, s16x4 b) { return __builtin_shufflevector(a, b, 0, 1, 2, 3, 4, 5, 6, 7); }
DEV f32x4 mfma16(s16x8 a, s16x8 b, f32x4 c) { return __builtin_amdgcn_mfma_f32_16x16x32_bf16(__builtin_bit_cast(bf16x8, a), __builtin_bit_cast(bf16x8, b), c, 0, 0, 0); }
DEV f32x16 mfma32(s16x8 a, s16x8 b, f32x16 c) { return __builtin_amdgcn_mfma_f32_32x32x16_bf16(__builtin_bit_cast(bf16x8, a), __builtin_bit_cast(bf16x8, b), c, 0, 0, 0); }
DEV f32x16 mfma32k8(s16x4 a, s16x4 b, f32x16 c) { return __builtin_amdgcn_mfma_f32_32x32x8bf16_1k(a, b, c, 0, 0, 0); }
DEV float xhalf_max(float m) { auto r = __builtin_amdgcn_permlane32_swap(__float_as_uint(m), __float_as_uint(m), false, false); return fmaxf(__uint_as_float(r[0]), __uint_as_float(r[1])); }
template <int CTRL> DEV float dpp_add(float x) { return x + __uint_as_float(__builtin_amdgcn_update_dpp(0u, __float_as_uint(x), CTRL, 0xf, 0xf, true)); }
DEV float sum_x16(float s) { auto r = __builtin_amdgcn_permlane16_swap(__float_as_uint(s), __float_as_uint(s), false, false); return __uint_as_float(r[0]) + __uint_as_float(r[1]); }
DEV float sum_x32(float s) { auto r = __builtin_amdgcn_permlane32_swap(__float_as_uint(s), __float_as_uint(s), false, false); return __uint_as_float(r[0]) + __uint_as_float(r[1]); }
DEV float sum_x16_x32(float s) { return sum_x32(sum_x16(s)); }
DEV float sum_x8(float s) { return dpp_add<0x128>(s); }
DEV float sum_row16(float s) { return dpp_add<0x140>(dpp_add<0x141>(dpp_add<0x4e>(dpp_add<0xb1>(s)))); }
DEV void ln_stats(float mean, float var, float& m_out, float& r_out) {
  m_out = rbf(mean);
  const float sd = rbf(__builtin_amdgcn_sqrtf(rbf(rbf(var) + 1e-5f)));
  r_out = __builtin_amdgcn_rcpf(sd);
}

struct Sync {
  GAS u64* flags; u64 tag;
  DEV void wait(int idx) const {
    GAS u64* p = flags + (idx < 0 ? 0 : idx) * 16;
    for (u32 spins = 0; spins < (1u << 19); ++spins) {
      const u64 v = __hip_atomic_load(p, __ATOMIC_RELAXED, __HIP_MEMORY_SCOPE_AGENT);
      if (__all(idx < 0 || v == tag)) break;
    }
  }
  DEV void post(int idx) const { __hip_atomic_store(flags + idx * 16, tag, __ATOMIC_RELAXED, __HIP_MEMORY_SCOPE_AGENT); }
};


DEV void colsum_job(const u16* wg, const u16* wu, char* ws, int G0, u32 lds0, int tid, int lane, int w) {
  if (G0 < 54) {
  u32x4 v[2][10];
#pragma unroll
  for (int sg = 0; sg < 2; ++sg) {
    int G = G0 + sg; if (G > 53) G = 53;
    const u16* W = (G < 27 ? wg + G * 64 : wu + (G - 27) * 64) + (lane & 7) * 8 + (size_t)(8 * w + (lane >> 3)) * FF;
#pragma unroll
    for (int i = 0; i < 10; ++i) v[sg][i] = *(const u32x4*)(W + (size_t)i * 64 * FF);
  }
#pragma unroll
  for (int sg = 0; sg < 2; ++sg) {
    float a[8];
#pragma unroll
    for (int e = 0; e < 8; ++e) a[e] = 0.f;
#pragma unroll
    for (int i = 0; i < 10; ++i)
#pragma unroll
      for (int e = 0; e < 4; ++e) { a[2 * e] += bf_lo(v[sg][i][e]); a[2 * e + 1] += bf_hi(v[sg][i][e]); }
#pragma unroll
    for (int e = 0; e < 8; ++e) { a[e] = sum_x16_x32(sum_x8(a[e])); }
    if (lane < 8) {
      LAS f32x4* p = (LAS f32x4*)(size_t)(lds0 + ((sg * 8 + w) * 64 + lane * 8) * 4);
      p[0] = (f32x4){a[0], a[1], a[2], a[3]}; p[1] = (f32x4){a[4], a[5], a[6], a[7]};
    }
  }
  }
  BAR();
  if (tid < 128) {
    const int sg = tid >> 6, c = tid & 63; float s = 0.f;
#pragma unroll
    for (int ww = 0; ww < 8; ++ww) s += *(LAS float*)(size_t)(lds0 + ((sg * 8 + ww) * 64 + c) * 4);
    if (G0 + sg < 54) __hip_atomic_store((GAS float*)(ws + WS_CS) + (G0 + sg) * 64 + c, s, __ATOMIC_RELAXED, __HIP_MEMORY_SCOPE_AGENT);
  }
  WAITV(0);
  BAR();
}

#define SCHED0() __builtin_amdgcn_sched_barrier(0)
#define SGB(mask, n) __builtin_amdgcn_sched_group_barrier(mask, n, 0)
#define PBAR() asm volatile("s_barrier" ::: "memory")
constexpr int M_MFMA = 0x8, M_DSR = 0x100;

constexpr u32 P1_TAB = 120 * 1024;
constexpr u32 KV_PL = 8256, KV_K = 81920, KV_V = KV_K + 3 * KV_PL;
constexpr u32 P3_A = 81920;
constexpr u32 P4_ST = 24576;
constexpr u32 P5_A = 65536;

struct Geo {
  int T, s;
  bool has1; int RT, ct;
  int QB, hd;
  bool own35; int rt3, ct3, nrow3, row3;
  bool has4; int R4, c4;
};

DEV void code_prefetch(u64 pc0, int lane, int part) {
  const char* p = (const char*)(pc0 & ~(u64)127) + part * (CODE_KB / 4) * 1024 + lane * 16;
  u32 acc = 0;
#pragma unroll
  for (int i = 0; i < CODE_KB / 4; ++i) acc ^= (*(const GAS u32x4*)(p + i * 1024))[0];
  asm volatile("" ::"v"(acc));
}

DEV void loader_main(const Geo& G, u64 pc0, const u16* x, const u16* wqkv, const u16* wo, const u16* wg, const u16* wu, const u16* wd, char* ws,
                     u32 L0, int tid, int lane, int lw) {
  const int l15 = lane & 15, g = lane >> 4;
  auto bn_voff = [&](int i8, int pitch, int colb) -> u32 {
    const int k = 8 * i8 + (lane >> 3);
    const int sw = ((k >> 1) & 1) | (((k >> 3) & 1) << 1);
    return (u32)(k * pitch + colb + ((((lane & 7) >> 1) ^ sw) * 32) + (lane & 1) * 16);
  };

  if (G.has1) {
    const u32x4 rA = mk_rsrc(x, SEQ * DM * 2), rB = mk_rsrc(wqkv, DM * 3 * DM * 2);
    u32 vA[4], vB[4];
#pragma unroll
    for (int ii = 0; ii < 4; ++ii) {
      const int i = lw * 4 + ii;
      const int row = 8 * i + (lane >> 3), c = (lane & 7) ^ ((row >> 1) & 7);
      vA[ii] = (u32)((128 * G.RT + row) * 1280 + c * 16);
      const int k = 4 * i + g;
      vB[ii] = (u32)(k * 3840 + G.ct * 256 + (((l15 >> 2) ^ (k & 3)) * 64) + (l15 & 3) * 16);
    }
    auto issue = [&](int c, int slot) {
      const u32 base = L0 + slot * 32768 + lw * 4096;
#pragma unroll
      for (int ii = 0; ii < 4; ++ii) dma(rA, vA[ii], c * 128, base + ii * 1024);
#pragma unroll
      for (int ii = 0; ii < 4; ++ii) dma(rB, vB[ii], c * (64 * 3840), base + 16384 + ii * 1024);
    };
    issue(0, 0); issue(1, 1);
    WAITV(8); BAR();
    issue(2, 2);
    WAITV(8);
    int slot = 4;
#pragma unroll 1
    for (int c = 0; c < 10; ++c) {
      BAR();
      if (c == 0) issue(3, 3);
      if (c < 6) { issue(c + 4, slot); slot = slot == 4 ? 0 : slot + 1; WAITV(16); }
      else if (c == 6) WAITV(8); else WAITV(0);
    }
    BAR(); BAR();
  } else { BAR(); BAR(); }

  const u32 vW0 = bn_voff(2 * lw, 1280, G.ct3 * 128), vW1 = bn_voff(2 * lw + 1, 1280, G.ct3 * 128);
  BAR();
  {
    const u32x4 rQ = mk_rsrc(ws + WS_QKV, SZ_QKV);
    if (G.QB > 0 || lw >= 2) {
      const int P0 = (5 * G.hd) >> 1;
#pragma unroll 1
      for (int i = 0; i < 6; ++i) {
        const int tv = i >= 3, j = i - 3 * tv;
        const u32 so = (u32)((80 + 80 * tv + P0 + j) * 32768 + (256 * (G.QB - 1) + 128 * lw) * 16), ld = L0 + (tv ? KV_V : KV_K) + j * KV_PL + lw * 2048;
        dma_c(rQ, lane * 16, so, ld); dma_c(rQ, lane * 16, so + 1024, ld + 1024);
      }
    }
    if (G.QB > 0 || lw >= 2) WAITV(6);
    BAR();
    WAITV(0);
    BAR();
  }
  if (!G.own35) {
    if (G.s == 31) code_prefetch(pc0, lane, lw);
    BAR(); BAR(); BAR();
    if (G.s == 31) code_prefetch(pc0, lane, lw);
  } else {
    {
      const u32x4 rW = mk_rsrc(wo, DM * DM * 2);
#pragma unroll 1
      for (int c = 0; c < 10; ++c) { dma_c(rW, vW0, c * (64 * 1280), L0 + c * 8192 + lw * 2048); dma_c(rW, vW1, c * (64 * 1280), L0 + c * 8192 + lw * 2048 + 1024); }
    }
    BAR();
    BAR();
    const u32x4 rO = mk_rsrc(ws + WS_O, SZ_O);
    u32 vA[3];
#pragma unroll
    for (int ii = 0; ii < 3; ++ii) {
      const int u = (lw * 3 + ii) * 64 + lane, pl = u / 48, un = (u - pl * 48) ^ (((pl >> 1) & 1) << 3);
      vA[ii] = 2 * un < G.nrow3 ? (u32)(pl * 16384 + (G.row3 + 2 * un) * 8) : OOB;
    }
    auto issue = [&](int c, int slot) {
#pragma unroll
      for (int ii = 0; ii < 3; ++ii) dma_c(rO, vA[ii], c * (16 * 16384), L0 + P3_A + slot * 12288 + (lw * 3 + ii) * 1024);
    };
#pragma unroll 1
    for (int c = 0; c < 4; ++c) issue(c, c);
    WAITV(6); BAR();
    issue(4, 4); issue(5, 5);
    WAITV(6);
#pragma unroll 1
    for (int i = 0; i < 5; ++i) {
      BAR();
      if (i < 2) { issue(2 * i + 6, 2 * i); issue(2 * i + 7, 2 * i + 1); WAITV(6); } else WAITV(0);
    }
    BAR();
    BAR();
  }

  if (G.has4) {
    const u32x4 rA = mk_rsrc(ws + WS_H, SZ_H), rG = mk_rsrc(wg, DM * FF * 2), rU = mk_rsrc(wu, DM * FF * 2);
    const u32 vN = bn_voff(lw, FF * 2, G.c4 * 128);
    auto issueB = [&](int t, int slot) {
      dma_c(rG, vN, t * (32 * FF * 2), L0 + slot * P4_ST + 16384 + lw * 1024);
      dma_c(rU, vN, t * (32 * FF * 2), L0 + slot * P4_ST + 20480 + lw * 1024);
    };
    const u32 vA = (u32)((256 * G.R4 + lane) * 16);
    auto issueA = [&](int t, int slot) {
#pragma unroll
      for (int ii = 0; ii < 4; ++ii) dma_c(rA, vA, (4 * t + lw) * 32768 + ii * 1024, L0 + slot * P4_ST + lw * 4096 + ii * 1024);
    };
#pragma unroll 1
    for (int t = 0; t < 5; ++t) issueB(t, t);
    BAR();
    issueA(0, 0); issueA(1, 1);
    WAITV(4); BAR();
    issueA(2, 2);
    WAITV(4);
    int slot = 5;
#pragma unroll 1
    for (int t = 0; t < 15; ++t) {
      BAR();
      if (t == 0) { issueA(3, 3); issueA(4, 4); }
      issueA(t + 5, slot); issueB(t + 5, slot); slot = slot == 5 ? 0 : slot + 1;
      if (t < 2) WAITV(14); else WAITV(18);
    }
    asm volatile("" ::: "memory");
    const int row = tid - 512;
    const __amdgpu_buffer_rsrc_t rS = mk_rs(ws + WS_HST, SZ_HST), rC = mk_rs(ws + WS_CS, SZ_CS);
    u32x2 pp[20];
#pragma unroll
    for (int i = 0; i < 20; ++i) pp[i] = __builtin_amdgcn_raw_buffer_load_b64(rS, (u32)(((G.R4 * 20 + i) * 256 + row) * 8), 0, 16);
    const u32 csv = __builtin_amdgcn_raw_buffer_load_b32(rC, (u32)((((row >> 6) & 1) * 27 + G.c4) * 256 + (row & 63) * 4), 0, 16);
    BAR(); WAITV(33);
    BAR(); WAITV(27);
    BAR(); WAITV(0);
    {
      f32x2 sm = {0.f, 0.f};
#pragma unroll
      for (int i = 0; i < 20; i += 2) sm += (f32x2){__uint_as_float(pp[i][0]), __uint_as_float(pp[i + 1][0])};
      const float mean = (sm[0] + sm[1]) * 0.05f;
      f32x2 m2 = {0.f, 0.f};
#pragma unroll
      for (int i = 0; i < 20; i += 2) {
        const f32x2 d = (f32x2){__uint_as_float(pp[i][0]), __uint_as_float(pp[i + 1][0])} - mean;
        m2 += (f32x2){__uint_as_float(pp[i][1]), __uint_as_float(pp[i + 1][1])} + d * d * 32.0f;
      }
      float m, r; ln_stats(mean, (m2[0] + m2[1]) * (1.0f / DM), m, r);
      *(LAS f32x2*)(size_t)(L0 + L_TAB + row * 8) = (f32x2){m, r};
      if (row < 128) *(LAS u32*)(size_t)(L0 + L_TAB + 2048 + row * 4) = csv;
    }
    BAR(); BAR(); BAR(); BAR();
  } else if (G.s >= 28) {
    const u32x4 rW = mk_rsrc(wd, FF * DM * 2);
#pragma unroll 1
    for (int i = 0; i < 135; ++i) {
      dma(rW, lane * 16, (u32)(((G.s - 28) * 540 + lw * 135 + i) * 1024), L0 + (lw * 16 + (i & 15)) * 1024);
      WAITV(24);
    }
    WAITV(0);
  }

  if (G.own35) {
    const u32x4 rW = mk_rsrc(wd, FF * DM * 2), rG = mk_rsrc(ws + WS_G, SZ_G);
    auto issueB = [&](int i, int slot) {
#pragma unroll
      for (int cc = 0; cc < 2; ++cc) {
        const int c = 2 * i + cc > 26 ? 26 : 2 * i + cc;
        dma_c(rW, vW0, c * (64 * 1280), L0 + slot * 16384 + cc * 8192 + lw * 2048);
        dma_c(rW, vW1, c * (64 * 1280), L0 + slot * 16384 + cc * 8192 + lw * 2048 + 1024);
      }
    };
#pragma unroll 1
    for (int i = 0; i < 4; ++i) issueB(i, i);
    u32 vA[3];
#pragma unroll
    for (int ii = 0; ii < 3; ++ii) {
      const int u = (lw * 3 + ii) * 64 + lane, pl = u / 96, tk = u - pl * 96;
      vA[ii] = tk < G.nrow3 ? (u32)(pl * 32768 + (G.row3 + tk) * 16) : OOB;
    }
    auto issueA = [&](int i, int slot) {
#pragma unroll
      for (int cc = 0; cc < 2; ++cc) {
        const int c = 2 * i + cc > 26 ? 26 : 2 * i + cc;
#pragma unroll
        for (int ii = 0; ii < 3; ++ii) dma_c(rG, vA[ii], c * (8 * 32768), L0 + P5_A + slot * 24576 + cc * 12288 + (lw * 3 + ii) * 1024);
      }
    };
    BAR();
    issueA(0, 0); issueA(1, 1);
    WAITV(6); BAR();
    issueA(2, 2);
    WAITV(6);
#pragma unroll 1
    for (int i = 0; i < 14; ++i) {
      BAR();
      if (i == 0) issueA(3, 3);
      if (i + 4 < 14) { issueA(i + 4, i & 3); issueB(i + 4, i & 3); }
      if (i == 0) WAITV(16); else if (i == 1) WAITV(20); else if (i < 10) WAITV(20); else if (i == 10) WAITV(10); else WAITV(0);
    }
    BAR();
  }
}


struct SideJob {
  u32x4 a0, a1, wv[3]; u32x2 a2;
  DEV void load(const Geo& G, const u16* x, const u16* wqkv, int lane, int hw) {
    const __amdgpu_buffer_rsrc_t rX = mk_rs(x, SEQ * DM * 2), rW = mk_rs(wqkv, DM * 3 * DM * 2);
    const int row = 8 * (int)blockIdx.x + 2 * hw + (lane >> 5);
    const u32 xo = (u32)(row * 1280 + (lane & 31) * 40);
    a0 = __builtin_amdgcn_raw_buffer_load_b128(rX, xo, 0, 0); a1 = __builtin_amdgcn_raw_buffer_load_b128(rX, xo + 16, 0, 0);
    a2 = __builtin_amdgcn_raw_buffer_load_b64(rX, xo + 32, 0, 0);
    const int job = G.RT * 15 + G.ct, range = job / 30, seg = job - range * 30;
#pragma unroll
    for (int i = 0; i < 3; ++i) {
      const int kr = 8 * i + (lane >> 3);
      wv[i] = __builtin_amdgcn_raw_buffer_load_b128(rW, (G.has1 && kr < 20) ? (u32)((80 * range + 20 * hw + kr) * 3840 + seg * 128 + (lane & 7) * 16) : OOB, 0, 0);
    }
  }
  DEV void store_rows(char* ws, int lane, int hw) {
    const int row = 8 * (int)blockIdx.x + 2 * hw + (lane >> 5);
    float v[20];
#pragma unroll
    for (int e = 0; e < 4; ++e) { v[2 * e] = bf_lo(a0[e]); v[2 * e + 1] = bf_hi(a0[e]); v[8 + 2 * e] = bf_lo(a1[e]); v[9 + 2 * e] = bf_hi(a1[e]); }
    v[16] = bf_lo(a2[0]); v[17] = bf_hi(a2[0]); v[18] = bf_lo(a2[1]); v[19] = bf_hi(a2[1]);
    auto red32 = [&](float t) { return sum_x16(sum_row16(t)); };
    float t0 = 0.f, t1 = 0.f;
#pragma unroll
    for (int e = 0; e < 20; e += 2) { t0 += v[e]; t1 += v[e + 1]; }
    const float mean = red32(t0 + t1) * (1.0f / DM);
    t0 = 0.f; t1 = 0.f;
#pragma unroll
    for (int e = 0; e < 20; e += 2) { const float d0 = v[e] - mean, d1 = v[e + 1] - mean; t0 += d0 * d0; t1 += d1 * d1; }
    float m, r; ln_stats(mean, red32(t0 + t1) * (1.0f / DM), m, r);
    __builtin_amdgcn_raw_buffer_store_b64((u32x2){__float_as_uint(m), __float_as_uint(r)}, mk_rs(ws + WS_XST, SZ_XST), (lane & 31) == 0 ? (u32)((row >> 1) * 128 + (row & 1) * 8) : OOB, 0, 16);
  }
  DEV void store_cols(const Geo& G, char* ws, int lane, int hw) {
    const int job = G.RT * 15 + G.ct, range = job / 30, seg = job - range * 30;
    float c[8];
#pragma unroll
    for (int e = 0; e < 4; ++e) { c[2 * e] = bf_lo(wv[0][e]) + bf_lo(wv[1][e]) + bf_lo(wv[2][e]); c[2 * e + 1] = bf_hi(wv[0][e]) + bf_hi(wv[1][e]) + bf_hi(wv[2][e]); }
#pragma unroll
    for (int e = 0; e < 8; ++e) c[e] = sum_x16_x32(sum_x8(c[e]));
    const __amdgpu_buffer_rsrc_t rC = mk_rs(ws + WS_QCS, SZ_QCS);
    const u32 co = (G.has1 && lane < 16) ? (u32)(((seg * 32 + range * 4 + hw) * 64 + (lane & 7) * 8 + (lane >> 3) * 4) * 4) : OOB;
    const f32x4 cv = (lane & 8) ? (f32x4){c[4], c[5], c[6], c[7]} : (f32x4){c[0], c[1], c[2], c[3]};
    __builtin_amdgcn_raw_buffer_store_b128(__builtin_bit_cast(u32x4, cv), rC, co, 0, 16);
  }
  DEV void post(const Sync& sy, int lane, int hw) {
    WAITV(0);
    if (lane == 0) __hip_atomic_store(sy.flags + (FL_SJ + 4 * blockIdx.x + hw) * 16, sy.tag, __ATOMIC_RELAXED, __HIP_MEMORY_SCOPE_AGENT);
  }
};

DEV void compute_main(const Geo& G, u64 pc0, const Sync& sy, const u16* x, const u16* wqkv, const u16* wg, const u16* wu, u16* out, char* ws, u32 L0, int tid, int lane, int w) {
  const int l15 = lane & 15, g = lane >> 4, q4 = l15 >> 2, p4 = lane & 3;
  const int r31 = lane & 31, h = lane >> 5, blk = g & 1;
  SideJob sj;
  if (w >= 4) sj.load(G, x, wqkv, lane, w - 4);
  if (G.has1) {
    if (w >= 4) {
      const int hw = w - 4;
      const u32x4 rF = mk_rsrc(ws + WS_FLAGS, 4000 * 128);
      const int seg0 = 2 * G.ct;
      u32 fo0 = [&] {
        if (lane < 32) return (u32)((FL_SJ + 64 * G.RT + 16 * hw + (lane & 15)) * 128);
        const int job = ((lane >> 2) & 7) * 30 + seg0 + (hw >> 1);
        const int RTj = job / 15, ctj = job - RTj * 15;
        const int bj = ctj == 7 ? (2 * (RTj >> 2) + ((RTj >> 1) & 1)) + 8 * (28 + (RTj & 1)) : (2 * (RTj >> 2) + (ctj >> 3)) + 8 * (4 * (ctj & 7) + (RTj & 3));
        return (u32)((FL_SJ + 4 * bj + (lane & 3)) * 128); }();
      u32 xo = lane < 32 ? (u32)((64 * G.RT + 16 * hw + (lane >> 1)) * 128 + (lane & 1) * 8) : OOB;
      u32 co = (u32)((((seg0 + (hw >> 1)) * 32 + 4 * (lane >> 3)) * 64 + 32 * (hw & 1) + 4 * (lane & 7)) * 4);
      asm volatile("" : "+v"(fo0), "+v"(xo), "+v"(co));
      PBAR();
      sj.store_rows(ws, lane, hw);
      PBAR();
      sj.store_cols(G, ws, lane, hw);
      PBAR(); PBAR();
      sj.post(sy, lane, hw);
      PBAR(); PBAR(); PBAR(); PBAR();
      u32x2 f0;
      asm volatile("buffer_load_dwordx2 %0, %1, %2, 0 offen sc1" : "=&v"(f0) : "v"(fo0), "s"(rF));
      PBAR();
      asm volatile("s_waitcnt vmcnt(0)" : "+v"(f0));
      {
        const u32 tl = (u32)sy.tag, th = (u32)(sy.tag >> 32);
        for (u32 spins = 0; spins < (1u << 19) && !__all(f0[0] == tl && f0[1] == th); ++spins)
          asm volatile("buffer_load_dwordx2 %0, %1, %2, 0 offen sc1\n\ts_waitcnt vmcnt(0)" : "=&v"(f0) : "v"(fo0), "s"(rF) : "memory");
      }
      asm volatile("" ::: "memory");
      const __amdgpu_buffer_rsrc_t rX = mk_rs(ws + WS_XST, SZ_XST), rC = mk_rs(ws + WS_QCS, SZ_QCS);
      const u32x2 xs = __builtin_amdgcn_raw_buffer_load_b64(rX, xo, 0, 16);
      u32x4 cp[4];
#pragma unroll
      for (int i = 0; i < 4; ++i) cp[i] = __builtin_amdgcn_raw_buffer_load_b128(rC, co, i * 256, 16);
      PBAR();
      PBAR();
      asm volatile("" ::: "memory");
      f32x4 c4 = __builtin_bit_cast(f32x4, cp[0]) + __builtin_bit_cast(f32x4, cp[1]) + (__builtin_bit_cast(f32x4, cp[2]) + __builtin_bit_cast(f32x4, cp[3]));
#pragma unroll
      for (int e = 0; e < 4; ++e) c4[e] = sum_x16_x32(sum_x8(c4[e]));
      if (lane < 32) *(LAS u32x2*)(size_t)(L0 + P1_TAB + (32 * hw + lane) * 8) = xs;
      if (lane < 8) *(LAS f32x4*)(size_t)(L0 + P1_TAB + 1024 + (32 * hw + 4 * lane) * 4) = c4;
      BAR();
    } else {
      const int wr = w >> 1, wc = w & 1;
      f32x16 acc[2][2];
#pragma unroll
      for (int i = 0; i < 2; ++i)
#pragma unroll
        for (int j = 0; j < 2; ++j)
#pragma unroll
          for (int r = 0; r < 16; ++r) acc[i][j][r] = 0.f;
      u32 xa[4], wa[2];
      {
        const int row = 64 * wr + r31, f = (row >> 1) & 7;
#pragma unroll
        for (int kk = 0; kk < 4; ++kk) xa[kk] = row * 128 + (((2 * kk + h) ^ f) * 16);
#pragma unroll
        for (int j = 0; j < 2; ++j) wa[j] = 16384 + (8 * h + q4) * 256 + (((2 * wc + j) ^ q4) * 64) + blk * 32 + 8 * p4;
      }
      s16x8 xA[2][2], wA[2][2], xB[2][2], wB[2][2];
#define P1_READ(X, W, base, ks) do { _Pragma("unroll") for (int kk = 0; kk < 2; ++kk) { \
        _Pragma("unroll") for (int j = 0; j < 2; ++j) { const u32 a_ = (base) + wa[j] + (2 * (ks) + kk) * 4096; W[kk][j] = cat(lds_tr(a_), lds_tr(a_ + 1024)); } \
        _Pragma("unroll") for (int i = 0; i < 2; ++i) X[kk][i] = lds_b128((base) + xa[2 * (ks) + kk] + i * 4096); } } while (0)
#define P1_MMA(X, W) do { _Pragma("unroll") for (int kk = 0; kk < 2; ++kk) _Pragma("unroll") for (int i = 0; i < 2; ++i) _Pragma("unroll") for (int j = 0; j < 2; ++j) \
        acc[i][j] = mfma32(W[kk][j], X[kk][i], acc[i][j]); } while (0)
#define P1_SCHED() do { _Pragma("unroll") for (int z = 0; z < 6; ++z) { SGB(M_MFMA, 1); SGB(M_DSR, 2); } SGB(M_MFMA, 2); SCHED0(); } while (0)
      BAR();
      P1_READ(xA, wA, L0, 0);
      u32 base = L0;
#pragma unroll 1
      for (int c = 0; c < 10; ++c) {
        PBAR(); SCHED0();
        P1_READ(xB, wB, base, 1);
        P1_MMA(xA, wA);
        P1_SCHED();
        base = base == L0 + 4 * 32768 ? L0 : base + 32768;
        P1_READ(xA, wA, base, 0);
        P1_MMA(xB, wB);
        P1_SCHED();
      }
      BAR();
      const float qs = G.ct < 5 ? 0.22360679775f * 1.44269504089f : 1.0f;
      const __amdgpu_buffer_rsrc_t rO = mk_rs(ws + WS_QKV, SZ_QKV);
#pragma unroll
      for (int i = 0; i < 2; ++i) {
        const int row = 64 * wr + 32 * i + r31;
        const f32x2 st = *(LAS f32x2*)(size_t)(L0 + P1_TAB + row * 8);
        const float rr = st[1] * qs, nmr = -st[0] * rr;
#pragma unroll
        for (int j = 0; j < 2; ++j) {
          u32 pk[4][2];
#pragma unroll
          for (int qd = 0; qd < 4; ++qd) {
            const f32x4 cj = *(LAS f32x4*)(size_t)(L0 + P1_TAB + 1024 + (64 * wc + 32 * j + 8 * qd + 4 * h) * 4);
            const f32x4 a4 = {acc[i][j][4 * qd], acc[i][j][4 * qd + 1], acc[i][j][4 * qd + 2], acc[i][j][4 * qd + 3]};
            const f32x4 v = a4 * rr + cj * nmr;
            pk[qd][0] = pk_bf16(v[0], v[1]); pk[qd][1] = pk_bf16(v[2], v[3]);
          }
#pragma unroll
          for (int o = 0; o < 4; o += 2) {
            auto s0 = __builtin_amdgcn_permlane32_swap(pk[o][0], pk[o + 1][0], false, false);
            auto s1 = __builtin_amdgcn_permlane32_swap(pk[o][1], pk[o + 1][1], false, false);
            __builtin_amdgcn_raw_buffer_store_b128((u32x4){s0[0], s1[0], s0[1], s1[1]}, rO, (u32)((16 * G.ct + 8 * wc + 4 * j + o + h) * 32768 + (128 * G.RT + row) * 16), 0, 16);
          }
        }
      }
      WAITV(0);
    }
    BAR();
    if (tid == 0) sy.post(FL_QKV + G.RT * 15 + G.ct);
  } else {
    if (w >= 4) { sj.store_rows(ws, lane, w - 4); sj.post(sy, lane, w - 4); }
    if (G.s == 31 && w < 4) code_prefetch(pc0, lane, w);
    const int job = 2 * G.T + G.s - 30;
    colsum_job(wg, wu, ws, 2 * job, L0 + 96 * 1024, tid, lane, w);
    if (tid == 0) sy.post(FL_CS + job);
  }

  {
    const int QB = G.QB, hd = G.hd;
    const int off = hd & 1, P0 = (5 * hd) >> 1;
    *(LAS u32x4*)(size_t)(L0 + KV_V + 3 * KV_PL + tid * 16) = (u32x4){0x00003f80u, 0u, 0x00003f80u, 0u};
    if (w == 0) {
      int idx = -1;
      if (lane < 24) {
        const int sec = lane >> 3, r4 = (lane >> 1) & 3, e = lane & 1;
        const int RT = 2 * QB - 2 + r4;
        if (RT >= 0 && !(sec == 0 && r4 < 2)) idx = FL_QKV + RT * 15 + 5 * sec + (20 * hd + 19 * e) / 128;
      }
      sy.wait(idx);
    }
    BAR();
    const int q = r31;
    s16x8 q1; s16x4 q2;
    {
      const __amdgpu_buffer_rsrc_t rQ = mk_rs(ws + WS_QKV, SZ_QKV);
      const u32 tok = (u32)(256 * QB + 32 * w + q) * 16;
      q1 = __builtin_bit_cast(s16x8, __builtin_amdgcn_raw_buffer_load_b128(rQ, (P0 + off + h) * 32768 + tok, 0, 16));
      q2 = __builtin_bit_cast(s16x4, __builtin_amdgcn_raw_buffer_load_b64(rQ, h ? OOB : (P0 + 2 - 2 * off) * 32768 + tok + 8 * off, 0, 16));
    }
    BAR();
    const u32 kadr = L0 + KV_K + (off + h) * KV_PL + (32 * w + q) * 16;
    const u32 k2adr = L0 + KV_K + (2 - 2 * off) * KV_PL + (32 * w + q) * 16 + 8 * off;
    const int jq = 4 * (g & 1) + p4;
    const int lq = jq < 5 ? jq + off : (jq < 7 ? jq + 1 : (off ? 0 : 5));
    const u32 vadr = L0 + KV_V + (lq >> 1) * KV_PL + (32 * w + 4 * h + q4) * 16 + (lq & 1) * 8;
    const f32x16 zero16 = {0, 0, 0, 0, 0, 0, 0, 0, 0, 0, 0, 0, 0, 0, 0, 0};
    const int qd = q - 4 * h;
    constexpr float NEG = -3.0e38f;
    u64 vm[16];
#pragma unroll
    for (int r = 0; r < 16; ++r) vm[r] = __builtin_amdgcn_ballot_w64((r & 3) + 8 * (r >> 2) <= qd);
#define VIS(r) __builtin_amdgcn_inverse_ballot_w64(vm[r])
    auto score = [&](int j, const f32x16& c) -> f32x16 {
      return mfma32k8(lds_b64(k2adr + j * 512), q2, mfma32(lds_b128(kadr + j * 512), q1, c));
    };
    auto pv = [&](int j, int ks, u32 p0, u32 p1, u32 p2, u32 p3, const f32x16& o) -> f32x16 {
      const u32 a = vadr + j * 512 + ks * 256;
      return mfma32(cat(lds_tr(a), lds_tr(a + 128)), __builtin_bit_cast(s16x8, (u32x4){p0, p1, p2, p3}), o);
    };
    f32x16 o = zero16;
    float m = NEG;
    const int jstart = QB > 0 ? 1 : (8 - w > 1 ? 8 - w : 1);
    if (QB > 0) {
      const f32x16 sf = score(0, zero16), sd = score(8, zero16);
#pragma unroll
      for (int r = 0; r < 16; r += 2) {
        const float e0 = VIS(r) ? sd[r] : sf[r], e1 = VIS(r + 1) ? sd[r + 1] : sf[r + 1];
        m = fmaxf(fmaxf(m, e0), e1);
      }
#pragma unroll
      for (int j = 1; j < 8; ++j) {
        const f32x16 sc = score(j, zero16);
#pragma unroll
        for (int r = 0; r < 16; r += 2) m = fmaxf(fmaxf(m, sc[r]), sc[r + 1]);
      }
    } else {
      const f32x16 sd = score(8, zero16);
#pragma unroll
      for (int r = 0; r < 16; ++r) m = fmaxf(m, VIS(r) ? sd[r] : NEG);
#pragma unroll 1
      for (int j = jstart; j < 8; ++j) {
        const f32x16 sc = score(j, zero16);
#pragma unroll
        for (int r = 0; r < 16; r += 2) m = fmaxf(fmaxf(m, sc[r]), sc[r + 1]);
      }
    }
    m = xhalf_max(m);
    BAR();
    asm volatile("" ::: "memory");
    f32x16 negm;
#pragma unroll
    for (int r = 0; r < 16; ++r) negm[r] = -m;
    auto full = [&](int j) {
      const f32x16 sc = score(j, negm);
      u32 pk[8];
#pragma unroll
      for (int i = 0; i < 8; ++i) pk[i] = pk_bf16(__builtin_amdgcn_exp2f(sc[2 * i]), __builtin_amdgcn_exp2f(sc[2 * i + 1]));
      o = pv(j, 0, pk[0], pk[1], pk[2], pk[3], o);
      o = pv(j, 1, pk[4], pk[5], pk[6], pk[7], o);
    };
    if (QB > 0) {
#pragma unroll
      for (int j = 1; j < 8; ++j) full(j);
    } else {
#pragma unroll 1
      for (int j = jstart; j < 8; ++j) full(j);
    }
    {
      const f32x16 sd = score(8, negm);
      f32x16 sf;
      if (QB > 0) sf = score(0, negm);
      else {
#pragma unroll
        for (int r = 0; r < 16; ++r) sf[r] = NEG;
      }
      u32 pd[8], pf[8];
#pragma unroll
      for (int i = 0; i < 8; ++i) {
        const u32 pk = pk_bf16(__builtin_amdgcn_exp2f(VIS(2 * i) ? sd[2 * i] : sf[2 * i]), __builtin_amdgcn_exp2f(VIS(2 * i + 1) ? sd[2 * i + 1] : sf[2 * i + 1]));
        const u32 mk = VIS(2 * i) ? (VIS(2 * i + 1) ? 0xffffffffu : 0xffffu) : 0u;
        pd[i] = pk & mk; pf[i] = pk ^ pd[i];
      }
      o = pv(8, 0, pd[0], pd[1], pd[2], pd[3], o);
      o = pv(8, 1, pd[4], pd[5], pd[6], pd[7], o);
      if (QB > 0) {
        o = pv(0, 0, pf[0], pf[1], pf[2], pf[3], o);
        o = pv(0, 1, pf[4], pf[5], pf[6], pf[7], o);
      }
    }
    auto r = __builtin_amdgcn_permlane32_swap(__float_as_uint(o[8]), __float_as_uint(o[8]), false, false);
    const float inv = __builtin_amdgcn_rcpf(__uint_as_float(r[1]));
    const __amdgpu_buffer_rsrc_t rO = mk_rs(ws + WS_O, SZ_O);
    const u32 tok = (u32)(256 * QB + 32 * w + q) * 8;
#pragma unroll
    for (int i = 0; i < 3; ++i) {
      const u32x2 v = {pk_bf16(o[4 * i] * inv, o[4 * i + 1] * inv), pk_bf16(o[4 * i + 2] * inv, o[4 * i + 3] * inv)};
      __builtin_amdgcn_raw_buffer_store_b64(v, rO, (i == 2 && h) ? OOB : (u32)((5 * hd + 2 * i + h) * 16384) + tok, 0, 16);
    }
    WAITV(0);
    BAR();
    if (tid == 0) sy.post(FL_ATT + QB * 32 + hd);
  }

  u32 hres[3][2][2];
  const int wr3 = (w >> 1) & 1, wc3 = w & 1, kh = (w >> 2) & 1;
  u32 nb_adr[2];
  {
    const int sw = ((q4 >> 1) & 1) | ((g & 1) << 1);
#pragma unroll
    for (int j = 0; j < 2; ++j) nb_adr[j] = kh * 8192 + (8 * g + q4) * 128 + (((2 * wc3 + j) ^ sw) * 32) + 8 * p4;
  }
#define N_MMA(X, W) do { _Pragma("unroll") for (int ks = 0; ks < 2; ++ks) _Pragma("unroll") for (int i = 0; i < 3; ++i) _Pragma("unroll") for (int j = 0; j < 2; ++j) \
      acc[i][j] = mfma16(W[ks][j], X[ks][i], acc[i][j]); } while (0)
#define N_REDUCE(base) do { \
    if (kh) { _Pragma("unroll") for (int i = 0; i < 3; ++i) _Pragma("unroll") for (int j = 0; j < 2; ++j) *(LAS f32x4*)(size_t)((base) + ((w & 3) * 6 + i * 2 + j) * 1024 + lane * 16) = acc[i][j]; } \
    BAR(); \
    if (!kh) { _Pragma("unroll") for (int i = 0; i < 3; ++i) _Pragma("unroll") for (int j = 0; j < 2; ++j) acc[i][j] += *(LAS f32x4*)(size_t)((base) + ((w & 3) * 6 + i * 2 + j) * 1024 + lane * 16); } } while (0)

  if (G.own35) {
    u32x2 xr[3][2];
    if (w < 4) {
      const __amdgpu_buffer_rsrc_t rX = mk_rs(x, SEQ * DM * 2);
#pragma unroll
      for (int i = 0; i < 3; ++i)
#pragma unroll
        for (int j = 0; j < 2; ++j) {
          const int r = 48 * wr3 + 16 * i + l15;
          xr[i][j] = __builtin_amdgcn_raw_buffer_load_b64(rX, r < G.nrow3 ? (u32)((G.row3 + r) * 1280 + (64 * G.ct3 + 32 * wc3 + 16 * j + 4 * g) * 2) : OOB, 0, 0);
        }
    }
    if (w == 0) sy.wait(lane < 32 ? FL_ATT + G.T * 32 + lane : -1);
    BAR();
    f32x4 acc[3][2];
#pragma unroll
    for (int i = 0; i < 3; ++i) { acc[i][0] = (f32x4){0, 0, 0, 0}; acc[i][1] = (f32x4){0, 0, 0, 0}; }
    u32 aadr[3];
#pragma unroll
    for (int i = 0; i < 3; ++i) {
      const int tok = 48 * wr3 + 16 * i + l15;
      aadr[i] = P3_A + kh * 12288 + (2 * g) * 768 + (((tok >> 1) ^ ((g & 1) << 3)) * 16) + (tok & 1) * 8;
    }
    s16x8 xA[2][3], wA[2][2], xB[2][3], wB[2][2];
#define P3_READ(X, W, sa, sb) do { _Pragma("unroll") for (int ks = 0; ks < 2; ++ks) { \
      _Pragma("unroll") for (int j = 0; j < 2; ++j) { const u32 a_ = (sb) + nb_adr[j] + ks * 4096; W[ks][j] = cat(lds_tr(a_), lds_tr(a_ + 512)); } \
      _Pragma("unroll") for (int i = 0; i < 3; ++i) { const u32 a_ = (sa) + aadr[i] + ks * 8 * 768; X[ks][i] = cat(lds_b64(a_), lds_b64(a_ + 768)); } } } while (0)
#define P3_SCHED() do { _Pragma("unroll") for (int z = 0; z < 10; ++z) { SGB(M_MFMA, 1); SGB(M_DSR, 2); } SGB(M_MFMA, 2); SCHED0(); } while (0)
    BAR();
    P3_READ(xA, wA, L0, L0);
#pragma unroll 1
    for (int st = 0; st < 4; st += 2) {
      BAR(); SCHED0();
      P3_READ(xB, wB, L0 + (st == 0 ? 1 : 0) * 24576, L0 + (st + 1) * 16384);
      N_MMA(xA, wA);
      P3_SCHED();
      BAR(); SCHED0();
      P3_READ(xA, wA, L0 + (st == 0 ? 2 : 1) * 24576, L0 + (st + 2) * 16384);
      N_MMA(xB, wB);
      P3_SCHED();
    }
    BAR();
    N_MMA(xA, wA);
    N_REDUCE(L0);
    if (w < 4) {
      const __amdgpu_buffer_rsrc_t rH = mk_rs(ws + WS_H, SZ_H), rS = mk_rs(ws + WS_HST, SZ_HST);
#pragma unroll
      for (int i = 0; i < 3; ++i) {
        float hv[8];
#pragma unroll
        for (int j = 0; j < 2; ++j) {
          const u32 a0 = pk_bf16(acc[i][j][0], acc[i][j][1]), a1 = pk_bf16(acc[i][j][2], acc[i][j][3]);
          hres[i][j][0] = pk_bf16(bf_lo(xr[i][j][0]) + bf_lo(a0), bf_hi(xr[i][j][0]) + bf_hi(a0));
          hres[i][j][1] = pk_bf16(bf_lo(xr[i][j][1]) + bf_lo(a1), bf_hi(xr[i][j][1]) + bf_hi(a1));
          hv[4 * j] = bf_lo(hres[i][j][0]); hv[4 * j + 1] = bf_hi(hres[i][j][0]); hv[4 * j + 2] = bf_lo(hres[i][j][1]); hv[4 * j + 3] = bf_hi(hres[i][j][1]);
        }
        auto s0 = __builtin_amdgcn_permlane16_swap(hres[i][0][0], hres[i][1][0], false, false);
        auto s1 = __builtin_amdgcn_permlane16_swap(hres[i][0][1], hres[i][1][1], false, false);
        const int r = 48 * wr3 + 16 * i + l15;
        const int oct = 8 * G.ct3 + 4 * wc3 + ((g & 1) << 1) + (g >> 1);
        __builtin_amdgcn_raw_buffer_store_b128((u32x4){s0[0], s1[0], s0[1], s1[1]}, rH, r < G.nrow3 ? (u32)(oct * 32768 + (G.row3 + r) * 16) : OOB, 0, 16);
        float sm = 0.f;
#pragma unroll
        for (int e = 0; e < 8; ++e) sm += hv[e];
        const float mw = sum_x16_x32(sm) * (1.0f / 32.0f);
        float m2 = 0.f;
#pragma unroll
        for (int e = 0; e < 8; ++e) { const float d = hv[e] - mw; m2 += d * d; }
        m2 = sum_x16_x32(m2);
        __builtin_amdgcn_raw_buffer_store_b64((u32x2){__float_as_uint(mw), __float_as_uint(m2)}, rS,
                                              (g == 0 && r < G.nrow3) ? (u32)(((G.T * 20 + 2 * G.ct3 + wc3) * 256 + 96 * G.rt3 + r) * 8) : OOB, 0, 16);
      }
      WAITV(0);
    }
    BAR();
    if (tid == 0) sy.post(FL_H + G.T * 30 + G.s);
  } else {
    const int job = 16 + 2 * G.T + G.s - 30;
    colsum_job(wg, wu, ws, job < 27 ? 2 * job : 54, L0 + 96 * 1024, tid, lane, w);
    if (tid == 0 && job < 27) sy.post(FL_CS + job);
  }

  if (G.has4) {
    if (w == 0) {
      int idx = -1;
      if (lane < 30) idx = FL_H + G.R4 * 30 + lane;
      else if (lane == 30) idx = FL_CS + (G.c4 >> 1);
      else if (lane == 31) idx = FL_CS + ((27 + G.c4) >> 1);
      sy.wait(idx);
    }
    BAR();
    const int wr = w >> 1, wc = w & 1;
    f32x4 ag[4][2], au[4][2];
#pragma unroll
    for (int i = 0; i < 4; ++i)
#pragma unroll
      for (int j = 0; j < 2; ++j) { ag[i][j] = (f32x4){0, 0, 0, 0}; au[i][j] = (f32x4){0, 0, 0, 0}; }
    const u32 xadr = g * 4096 + (64 * wr + l15) * 16;
    u32 wadr[2];
#pragma unroll
    for (int j = 0; j < 2; ++j) wadr[j] = 16384 + (8 * g + q4) * 128 + (((2 * wc + j) ^ (((q4 >> 1) & 1) | ((g & 1) << 1))) * 32) + 8 * p4;
    s16x8 xA[4], gA[2], uA[2], xB[4], gB[2], uB[2];
#define P4_READ(X, G_, U_, base) do { _Pragma("unroll") for (int j = 0; j < 2; ++j) { const u32 a_ = (base) + wadr[j]; \
      G_[j] = cat(lds_tr(a_), lds_tr(a_ + 512)); U_[j] = cat(lds_tr(a_ + 4096), lds_tr(a_ + 4096 + 512)); } \
      _Pragma("unroll") for (int i = 0; i < 4; ++i) X[i] = lds_b128((base) + xadr + i * 256); } while (0)
#define P4_MMA(X, G_, U_) do { _Pragma("unroll") for (int i = 0; i < 4; ++i) _Pragma("unroll") for (int j = 0; j < 2; ++j) { \
      ag[i][j] = mfma16(G_[j], X[i], ag[i][j]); au[i][j] = mfma16(U_[j], X[i], au[i][j]); } } while (0)
#define P4_SCHED() do { _Pragma("unroll") for (int z = 0; z < 12; ++z) { SGB(M_MFMA, 1); SGB(M_DSR, 1); } SGB(M_MFMA, 4); SCHED0(); } while (0)
    BAR();
    P4_READ(xA, gA, uA, L0);
    u32 base = L0 + P4_ST;
#pragma unroll 1
    for (int t = 0; t < 20; t += 2) {
      PBAR(); SCHED0();
      P4_READ(xB, gB, uB, base);
      P4_MMA(xA, gA, uA);
      P4_SCHED();
      base = base == L0 + 5 * P4_ST ? L0 : base + P4_ST;
      PBAR(); SCHED0();
      P4_READ(xA, gA, uA, base);
      P4_MMA(xB, gB, uB);
      P4_SCHED();
      base = base == L0 + 5 * P4_ST ? L0 : base + P4_ST;
    }
    BAR();
    const __amdgpu_buffer_rsrc_t rH = mk_rs(ws + WS_G, SZ_G);
    f32x4 cg[2], cu[2];
#pragma unroll
    for (int j = 0; j < 2; ++j) {
      cg[j] = *(LAS f32x4*)(size_t)(L0 + L_TAB + 2048 + (32 * wc + 16 * j + 4 * g) * 4);
      cu[j] = *(LAS f32x4*)(size_t)(L0 + L_TAB + 2048 + 256 + (32 * wc + 16 * j + 4 * g) * 4);
    }
#pragma unroll
    for (int i = 0; i < 4; ++i) {
      const int row = 64 * wr + 16 * i + l15;
      const f32x2 st = *(LAS f32x2*)(size_t)(L0 + L_TAB + row * 8);
      const float r2 = st[1] * st[1];
      u32 pk[2][2];
#pragma unroll
      for (int j = 0; j < 2; ++j) {
        f32x4 a = ag[i][j] - cg[j] * st[0];
        const f32x4 u = (au[i][j] - cu[j] * st[0]) * r2;
#pragma unroll
        for (int e = 0; e < 4; ++e) a[e] = fmaxf(a[e], 0.f) * u[e];
        pk[j][0] = pk_bf16(a[0], a[1]); pk[j][1] = pk_bf16(a[2], a[3]);
      }
      auto s0 = __builtin_amdgcn_permlane16_swap(pk[0][0], pk[1][0], false, false);
      auto s1 = __builtin_amdgcn_permlane16_swap(pk[0][1], pk[1][1], false, false);
      const int oct = 8 * G.c4 + 4 * wc + ((g & 1) << 1) + (g >> 1);
      __builtin_amdgcn_raw_buffer_store_b128((u32x4){s0[0], s1[0], s0[1], s1[1]}, rH, (u32)(oct * 32768 + (256 * G.R4 + row) * 16), 0, 16);
    }
    WAITV(0);
    BAR();
    if (tid == 0) sy.post(FL_FF + G.R4 * 27 + G.c4);
  }

  if (G.own35) {
    if (w == 0) sy.wait(lane < 27 ? FL_FF + G.T * 27 + lane : -1);
    BAR();
    f32x4 acc[3][2];
#pragma unroll
    for (int i = 0; i < 3; ++i) { acc[i][0] = (f32x4){0, 0, 0, 0}; acc[i][1] = (f32x4){0, 0, 0, 0}; }
    const u32 aadr = P5_A + kh * 12288 + g * 1536 + (48 * wr3 + l15) * 16;
    s16x8 xA[2][3], wA[2][2], xB[2][3], wB[2][2];
#define P5_READ(X, W, sa, sb) do { _Pragma("unroll") for (int ks = 0; ks < 2; ++ks) { \
      _Pragma("unroll") for (int j = 0; j < 2; ++j) { const u32 a_ = (sb) + nb_adr[j] + ks * 4096; W[ks][j] = cat(lds_tr(a_), lds_tr(a_ + 512)); } \
      _Pragma("unroll") for (int i = 0; i < 3; ++i) X[ks][i] = lds_b128((sa) + aadr + ks * 4 * 1536 + i * 256); } } while (0)
#define P5_SCHED() do { _Pragma("unroll") for (int z = 0; z < 12; ++z) { SGB(M_MFMA, 1); SGB(M_DSR, 1); } SGB(M_DSR, 2); SCHED0(); } while (0)
    BAR();
    P5_READ(xA, wA, L0, L0);
    u32 sl = 1;
#pragma unroll 1
    for (int i = 0; i < 12; i += 2) {
      BAR(); SCHED0();
      P5_READ(xB, wB, L0 + sl * 24576, L0 + sl * 16384);
      N_MMA(xA, wA);
      P5_SCHED();
      sl = (sl + 1) & 3;
      BAR(); SCHED0();
      P5_READ(xA, wA, L0 + sl * 24576, L0 + sl * 16384);
      N_MMA(xB, wB);
      P5_SCHED();
      sl = (sl + 1) & 3;
    }
    BAR(); SCHED0();
    P5_READ(xB, wB, L0 + sl * 24576, L0 + sl * 16384);
    N_MMA(xA, wA);
    P5_SCHED();
    BAR();
    if (!kh) N_MMA(xB, wB);
    N_REDUCE(L0);
    if (w < 4) {
#pragma unroll
      for (int i = 0; i < 3; ++i) {
        u32 pk[2][2];
#pragma unroll
        for (int j = 0; j < 2; ++j) {
          const u32 a0 = pk_bf16(acc[i][j][0], acc[i][j][1]), a1 = pk_bf16(acc[i][j][2], acc[i][j][3]);
          pk[j][0] = pk_bf16(bf_lo(hres[i][j][0]) + bf_lo(a0), bf_hi(hres[i][j][0]) + bf_hi(a0));
          pk[j][1] = pk_bf16(bf_lo(hres[i][j][1]) + bf_lo(a1), bf_hi(hres[i][j][1]) + bf_hi(a1));
        }
        auto s0 = __builtin_amdgcn_permlane16_swap(pk[0][0], pk[1][0], false, false);
        auto s1 = __builtin_amdgcn_permlane16_swap(pk[0][1], pk[1][1], false, false);
        const int r = 48 * wr3 + 16 * i + l15;
        const int oct = 8 * G.ct3 + 4 * wc3 + ((g & 1) << 1) + (g >> 1);
        __builtin_amdgcn_raw_buffer_store_b128((u32x4){s0[0], s1[0], s0[1], s1[1]}, mk_rs(out, SEQ * DM * 2), r < G.nrow3 ? (u32)((G.row3 + r) * (DM * 2) + oct * 16) : OOB, 0, 0);
      }
    }
  }
  if (blockIdx.x == 0 && w == 0) {
#pragma unroll 1
    for (int i = 0; i < 4; ++i) sy.wait(FL_ATT + i * 64 + lane);
    if (lane == 0) __hip_atomic_store((GAS u64*)(ws + WS_EPOCH), sy.tag, __ATOMIC_RELAXED, __HIP_MEMORY_SCOPE_AGENT);
  }
}

__global__ __launch_bounds__(768, 3) void fused_block(const u16* __restrict__ x, const u16* __restrict__ wqkv, const u16* __restrict__ wo,
                                                      const u16* __restrict__ wg, const u16* __restrict__ wu, const u16* __restrict__ wd,
                                                      u16* __restrict__ out, char* __restrict__ ws) {
  u64 pc0; asm volatile("s_getpc_b64 %0" : "=s"(pc0));
  extern __shared__ __attribute__((aligned(16))) char smem[];
  const u32 L0 = (u32)(size_t)(LAS char*)smem;
  const int tid = threadIdx.x, lane = tid & 63;
  const int w = __builtin_amdgcn_readfirstlane(tid >> 6);
  const int b = blockIdx.x;
  Geo G;
  G.T = b & 7; G.s = b >> 3;
  G.has1 = G.s < 30;
  if (G.s < 28) { G.RT = 4 * (G.T >> 1) + (G.s & 3); G.ct = 8 * (G.T & 1) + (G.s >> 2); }
  else { G.RT = 4 * (G.T >> 1) + 2 * (G.T & 1) + (G.s & 1); G.ct = 7; }
  G.QB = G.s >> 2; G.hd = 4 * G.T + (G.s & 3);
  G.own35 = G.s < 30; G.rt3 = G.s / 10; G.ct3 = G.s - G.rt3 * 10; G.nrow3 = G.rt3 < 2 ? 96 : 64; G.row3 = 256 * G.T + 96 * G.rt3;
  G.R4 = 4 * (G.T & 1) + (G.s & 3); G.c4 = 7 * (G.T >> 1) + (G.s >> 2); G.has4 = G.s < 28 && G.c4 < 27;
  if (w >= 8) {
    loader_main(G, pc0, x, wqkv, wo, wg, wu, wd, ws, L0, tid, lane, w - 8);
  } else {
    Sync sy; sy.flags = (GAS u64*)(ws + WS_FLAGS); sy.tag = 0;
    if (w == 0 || w >= 4) {
      const u64 e = __hip_atomic_load((GAS u64*)(ws + WS_EPOCH), __ATOMIC_RELAXED, __HIP_MEMORY_SCOPE_AGENT) + 1;
      sy.tag = ((u64)(u32)__builtin_amdgcn_readfirstlane((u32)(e >> 32)) << 32) | (u32)__builtin_amdgcn_readfirstlane((u32)e);
    }
    compute_main(G, pc0, sy, x, wqkv, wg, wu, out, ws, L0, tid, lane, w);
  }
}

extern "C" void kernel_launch(void* const* d_in, const int* in_sizes, int n_in, void* d_out, int out_size, void* d_ws, size_t ws_size, hipStream_t stream) {
  (void)in_sizes; (void)n_in; (void)out_size; (void)ws_size;
  const u16* x = (const u16*)d_in[0]; const u16* wqkv = (const u16*)d_in[1]; const u16* wo = (const u16*)d_in[2];
  const u16* wg = (const u16*)d_in[4]; const u16* wu = (const u16*)d_in[5]; const u16* wd = (const u16*)d_in[6];
  (void)hipFuncSetAttribute((const void*)fused_block, hipFuncAttributeMaxDynamicSharedMemorySize, LDS_BYTES);
  fused_block<<<256, NTHR, LDS_BYTES, stream>>>(x, wqkv, wo, wg, wu, wd, (u16*)d_out, (char*)d_ws);
}
```
